```python
import torch

def _softmax(x, axis=-1):
    return torch.softmax(x, dim=axis)

def _gelu(x):
    return torch.nn.functional.gelu(x, approximate='tanh')

def _silu(x):
    return torch.nn.functional.silu(x)

def _sigmoid(x):
    return torch.sigmoid(x)

def _layernorm(x, axis=-1, eps=1e-05):
    m = x.mean(dim=axis, keepdim=True)
    v = x.var(dim=axis, keepdim=True, unbiased=False)
    return (x - m) / torch.sqrt(v + eps)

def _scatter_add(tgt, idx, src, axis=0):
    return tgt.clone().index_add_(axis, idx, src)

def _segment_sum(src, seg, n):
    out = torch.zeros((n,) + src.shape[1:], dtype=src.dtype)
    return out.index_add_(0, seg, src)

def _pad(x, pw):
    flat = tuple((v for pair in reversed(pw) for v in pair))
    return torch.nn.functional.pad(x, flat)
kernel_name = 'recurrent_scan__memory_aug_recurrent_mem_tr__glu_gated_bilinear__prompt'

def setup_inputs():

    _w = lambda a: (lambda t: t.to(torch.bfloat16) if t.is_floating_point() else t)(torch.as_tensor(a))

    def _A_setup():
        B, S, Dm = (1, 2048, 512)
        H, Dh = (32, 128)
        M, seg = (8, 32)
        seg = min(seg, S)
        S = S // seg * seg
        return {'x': _w((lambda *s: torch.randn(*s, dtype=torch.bfloat16) * 0.1)(B, S, Dm)), 'mem0': _w((lambda *s: torch.randn(*s, dtype=torch.bfloat16) * 1.6)(B, M, Dm)), 'Wq': _w((lambda *s: torch.randn(*s, dtype=torch.bfloat16) * 0.8)(Dm, H, Dh) * Dm ** (-0.5)), 'Wk': _w((lambda *s: torch.randn(*s, dtype=torch.bfloat16) * 0.8)(Dm, H, Dh) * Dm ** (-0.5)), 'Wv': _w((lambda *s: torch.randn(*s, dtype=torch.bfloat16) * 1.6)(Dm, H, Dh) * Dm ** (-0.5)), 'Wo': _w((lambda *s: torch.randn(*s, dtype=torch.bfloat16) * 1.6)(H, Dh, Dm) * (H * Dh) ** (-0.5)), 'seg_len': seg}
    _A_in = _A_setup()

    def _B_setup():
        B, S, D, F = (1, 2048, 512, 1792)
        return {'x': _w((lambda *s: torch.randn(*s, dtype=torch.bfloat16) * 0.1)(B, S, D)), 'w_gate': _w((lambda *s: torch.randn(*s, dtype=torch.bfloat16) * 0.4)(D, F) * D ** (-0.5)), 'w_up': _w((lambda *s: torch.randn(*s, dtype=torch.bfloat16) * 0.4)(D, F) * D ** (-0.5)), 'w_down': _w((lambda *s: torch.randn(*s, dtype=torch.bfloat16) * 0.4)(F, D) * F ** (-0.5))}
    _B_in = _B_setup()
    return {'A_x': _A_in['x'], 'A_mem0': _A_in['mem0'], 'A_Wq': _A_in['Wq'], 'A_Wk': _A_in['Wk'], 'A_Wv': _A_in['Wv'], 'A_Wo': _A_in['Wo'], 'A_seg_len': _A_in['seg_len'], 'B_x': _B_in['x'], 'B_w_gate': _B_in['w_gate'], 'B_w_up': _B_in['w_up'], 'B_w_down': _B_in['w_down']}

def reference(A_x, A_mem0, A_Wq, A_Wk, A_Wv, A_Wo, A_seg_len, B_x, B_w_gate, B_w_up, B_w_down):

    def _A_ref(x, mem0, Wq, Wk, Wv, Wo, seg_len):
        B, S, Dm = x.shape
        H, Dh = (Wq.shape[1], Wq.shape[2])
        M = mem0.shape[1]
        scale = Dh ** (-0.5)
        n_seg = S // seg_len
        L = M + seg_len
        idx = torch.arange(L)
        mask = (idx[:, None] < M) | (idx[:, None] >= idx[None, :])
        mem = mem0
        outs = []
        for s in range(n_seg):
            xs = x[:, s * seg_len:(s + 1) * seg_len, :].clone()
            h = torch.concatenate([mem, xs], axis=1)
            q = torch.einsum('btd,dhe->bhte', h, Wq)
            k = torch.einsum('btd,dhe->bhte', h, Wk)
            v = torch.einsum('btd,dhe->bhte', h, Wv)
            scores = torch.einsum('bhqe,bhke->bhqk', q, k) * scale
            scores = torch.where(mask[None, None], scores, -1000000000.0)
            p = _softmax(scores, axis=-1)
            o = torch.einsum('bhqk,bhke->bhqe', p, v)
            o = torch.einsum('bhte,hed->btd', o, Wo)
            mem = o[:, :M, :].clone()
            outs.append(o[:, M:, :])
        return torch.concatenate(outs, axis=1)

    def _B_ref(x, w_gate, w_up, w_down):
        g = torch.einsum('bsd,df->bsf', x, w_gate)
        u = torch.einsum('bsd,df->bsf', x, w_up)
        h = g * u
        return torch.einsum('bsf,fd->bsd', h, w_down)
    x = A_x
    xn = _layernorm(x, axis=-1)
    s = _A_ref(xn, A_mem0, A_Wq, A_Wk, A_Wv, A_Wo, A_seg_len)
    g = _silu(_B_ref(xn, B_w_gate, B_w_up, B_w_down))
    return x + s * g
```

```cpp
#include <hip/hip_runtime.h>
#include <hip/hip_bf16.h>
#include <stdint.h>

typedef unsigned u32;
typedef unsigned short u16;
typedef unsigned long long u64;
typedef u32 u32x2 __attribute__((ext_vector_type(2)));
typedef u32 u32x4 __attribute__((ext_vector_type(4)));
typedef u32 u32x8 __attribute__((ext_vector_type(8)));
typedef float f32x2 __attribute__((ext_vector_type(2)));
typedef float f32x4 __attribute__((ext_vector_type(4)));
typedef __bf16 bf16x2 __attribute__((ext_vector_type(2)));
typedef __bf16 bf16x8 __attribute__((ext_vector_type(8)));
typedef __bf16 bf16x16 __attribute__((ext_vector_type(16)));
typedef short s16x4 __attribute__((ext_vector_type(4)));
#define LAS __attribute__((address_space(3)))
#define GAS __attribute__((address_space(1)))
#define DEV __device__ __forceinline__

constexpr int S_ = 2048, DM = 512, NH = 32, DH = 128, NM = 8, SEG = 32, NSEG = 64, FF = 1792;
constexpr size_t WS_CTRL = 0;
#define RING 4
constexpr size_t WS_BASE = 160u << 20;
constexpr size_t WS_PB = WS_BASE;
constexpr size_t WS_VB = WS_PB + 4 * 524288;
constexpr size_t WS_MB = WS_VB + 4 * 524288;
constexpr size_t WS_SB = WS_MB + 16 * 16384;
constexpr size_t WS_TB = WS_SB + 16 * 16384;
constexpr size_t WS_INIT_END = WS_TB + 16384;
constexpr size_t WS_XF = 4u << 20;
constexpr size_t WS_QF = 8u << 20;
constexpr size_t WS_KF = 24u << 20;
constexpr size_t WS_VF = 40u << 20;
constexpr size_t WS_KM = 56u << 20;
constexpr size_t WS_VM = 60u << 20;
constexpr size_t WS_H = 64u << 20;
constexpr size_t WS_O = 80u << 20;
constexpr int C_ABORT = 0, C_EPOCH = 1;
constexpr int C_POL = 16;
constexpr int C_ARM = 9472;
constexpr int C_KMF = 64;
constexpr int C_KVF = 4096;
constexpr int C_KV2 = 8448;
constexpr int C_QF = 4608;
constexpr int C_HF = 4864;
constexpr int C_XF = 8192;
constexpr int C_OF = 6144;

#define OSL 4
constexpr u32 SENT = 0xFFFFFFFFu;
constexpr u32 SPIN_CAP = 1u << 21;

struct Params {
  const u16 *x, *mem0, *Wq, *Wk, *Wv, *Wo, *wg, *wu, *wd;
  u16* out;
  char* ws;
};

DEV u32 pk(float a, float b) { f32x2 v = {a, b}; return __builtin_bit_cast(u32, __builtin_convertvector(v, bf16x2)); }
DEV float bf2f(u16 v) { return __builtin_bit_cast(float, (u32)v << 16); }
DEV u16 f2bf(float a) { return (u16)(pk(a, 0.f) & 0xffff); }
DEV f32x4 smf(u32x4 a, u32x8 b, f32x4 c, int idx) {
  return __builtin_amdgcn_smfmac_f32_16x16x64_bf16(__builtin_bit_cast(bf16x8, a), __builtin_bit_cast(bf16x16, b), c, idx, 0, 0);
}
DEV f32x4 mfma32(u32x4 a, u32x4 b, f32x4 c) {
  return __builtin_amdgcn_mfma_f32_16x16x32_bf16(__builtin_bit_cast(bf16x8, a), __builtin_bit_cast(bf16x8, b), c, 0, 0, 0);
}
DEV u32 fu(float a) { return __builtin_bit_cast(u32, a); }
DEV float uf(u32 a) { return __builtin_bit_cast(float, a); }
DEV float hsum32(float a, float b) { auto r = __builtin_amdgcn_permlane32_swap(fu(a), fu(b), false, false); return uf(r[0]) + uf(r[1]); }
DEV float hsum16(float a, float b) { auto r = __builtin_amdgcn_permlane16_swap(fu(a), fu(b), false, false); return uf(r[0]) + uf(r[1]); }
DEV float xmax32(float a) { auto r = __builtin_amdgcn_permlane32_swap(fu(a), fu(a), false, false); return fmaxf(uf(r[0]), uf(r[1])); }
DEV float xmax16(float a) { auto r = __builtin_amdgcn_permlane16_swap(fu(a), fu(a), false, false); return fmaxf(uf(r[0]), uf(r[1])); }
DEV u32 nosent(u32 x) { return x < 0xFFFFFFFEu ? x : 0xFFFFFFFEu; }
DEV bool chunk_bad(u32x4 v) { u32 m = max(max(v.x, v.y), max(v.z, v.w)); return m == SENT; }
DEV bool any_lane(bool b) { return __builtin_amdgcn_ballot_w64(b) != 0; }

#define CBAR() asm volatile("" ::: "memory")
#define GLD4(dst, voff, sbase) asm volatile("global_load_dwordx4 %0, %1, %2 sc1" : "=v"(dst) : "v"(voff), "s"(sbase) : "memory")
#define GLD4O(dst, voff, sbase, OFF) asm volatile("global_load_dwordx4 %0, %1, %2 offset:" #OFF " sc1" : "=v"(dst) : "v"(voff), "s"(sbase) : "memory")
#define GLD1(dst, voff, sbase) asm volatile("global_load_dword %0, %1, %2 sc1" : "=v"(dst) : "v"(voff), "s"(sbase) : "memory")
#define GST4(voff, val, sbase) asm volatile("global_store_dwordx4 %0, %1, %2 sc1\n\ts_nop 1" :: "v"(voff), "v"(val), "s"(sbase) : "memory")
#define GST4O(voff, val, sbase, OFF) asm volatile("global_store_dwordx4 %0, %1, %2 offset:" #OFF " sc1\n\ts_nop 1" :: "v"(voff), "v"(val), "s"(sbase) : "memory")
#define GST2(voff, val, sbase) asm volatile("global_store_dwordx2 %0, %1, %2 sc1" :: "v"(voff), "v"(val), "s"(sbase) : "memory")
#define GST1(voff, val, sbase) asm volatile("global_store_dword %0, %1, %2 sc1" :: "v"(voff), "v"(val), "s"(sbase) : "memory")
#define GLD4X2W(d0, d1, voff, sbase) asm volatile("global_load_dwordx4 %0, %2, %3 sc1\n\tglobal_load_dwordx4 %1, %2, %3 offset:1024 sc1\n\ts_waitcnt vmcnt(0)" : "=&v"(d0), "=&v"(d1) : "v"(voff), "s"(sbase) : "memory")
#define LDSFLAG(addr, val, OFF) asm volatile("ds_write_b32 %0, %1 offset:%2" :: "v"(addr), "v"(val), "i"(OFF) : "memory")

constexpr int M_OFF = 0, Q_OFF = 8192, K_OFF = 10496, O_OFF = 12800, RED_OFF = 8192, ST_OFF = 16896, FL_OFF = 16960;
constexpr int FQ = FL_OFF, FK = FL_OFF + 4, FO = FL_OFF + 8, FR = FL_OFF + 12, FS = FL_OFF + 16;
constexpr int DUMP_OFF = 17408, T_OFF = 20480, KV_OFF = 65536, WSC_OFF = 98304, STAMP_OFF = 98304;
constexpr int QS = 272;
constexpr int LDS_BYTES = 160 * 1024;

template <int NT, int NS>
DEV void w_issue(u32x4 (&raw)[NS][2 * NT], const u16* base, int rs, int lane, int rot32) {
#pragma unroll
  for (int t = 0; t < NS; ++t)
#pragma unroll
    for (int u = 0; u < 2 * NT; ++u) {
      const int half = (u >= NT) ? 1 : 0, row = (lane + 64 * u) / (2 * NT) - 32 * half, cc = lane % (2 * NT);
      const u16* bt = base + (size_t)(32 * ((2 * t + half + rot32) & (2 * NS - 1))) * rs;
      raw[t][u] = *(const GAS u32x4*)(bt + row * rs + cc * 8);
    }
}
DEV u32x2 trd(int a) { return __builtin_bit_cast(u32x2, __builtin_amdgcn_ds_read_tr16_b64_v4i16((LAS s16x4*)(size_t)a)); }
template <int NT, int NS>
DEV void w_convert(u32x8 (&W)[NT][NS], u32x4 (&raw)[NS][2 * NT], int scr, int lane) {
  constexpr int RSB = NT * 32;
  const int g = lane >> 4, q4 = (lane & 15) >> 2, p = lane & 3;
  const int ra = scr + (16 * (g >> 1) + 4 * (g & 1) + q4) * RSB + p * 8;
#pragma unroll
  for (int t = 0; t < NS; ++t) {
#pragma unroll
    for (int u = 0; u < 2 * NT; ++u) *(LAS u32x4*)(size_t)(scr + (lane + 64 * u) * 16) = raw[t][u];
#pragma unroll
    for (int nt = 0; nt < NT; ++nt) {
      u32x2 X = trd(ra + nt * 32), Y = trd(ra + nt * 32 + 8 * RSB), X2 = trd(ra + nt * 32 + 32 * RSB), Y2 = trd(ra + nt * 32 + 40 * RSB);
      W[nt][t] = u32x8{X.x, Y.x, X.y, Y.y, X2.x, Y2.x, X2.y, Y2.y};
    }
    asm volatile("s_waitcnt lgkmcnt(0)" ::: "memory");
  }
}
DEV void w_convert3(u32x8 (&W)[16], u32x4 (&raw)[8][4], int scr, int lane) {
  const int g = lane >> 4, q4 = (lane & 15) >> 2, p = lane & 3;
  const int ra = scr + (16 * (g >> 1) + 4 * (g & 1) + q4) * 64 + p * 8;
#pragma unroll
  for (int t = 0; t < 8; ++t) {
#pragma unroll
    for (int u = 0; u < 4; ++u) *(LAS u32x4*)(size_t)(scr + (lane + 64 * u) * 16) = raw[t][u];
#pragma unroll
    for (int hf = 0; hf < 2; ++hf) {
      const int a = ra + (hf ^ ((g >> 1) & 1)) * 8 * 64;
      u32x2 X = trd(a), Y = trd(a + 32), X2 = trd(a + 32 * 64), Y2 = trd(a + 32 * 64 + 32);
      W[2 * t + hf] = u32x8{X.x, Y.x, X.y, Y.y, X2.x, Y2.x, X2.y, Y2.y};
    }
    asm volatile("s_waitcnt lgkmcnt(0)" ::: "memory");
  }
}
DEV void w_convert2(u32x8 (&W)[2][4], u32x4 (&raw)[2][8], int scr, int lane) {
  const int g = lane >> 4, q4 = (lane & 15) >> 2, p = lane & 3;
  const int ra = scr + (16 * (g & 1) + 4 * (g >> 1) + q4) * 128 + p * 8;
#pragma unroll
  for (int t = 0; t < 2; ++t) {
#pragma unroll
    for (int u = 0; u < 8; ++u) *(LAS u32x4*)(size_t)(scr + (lane + 64 * u) * 16) = raw[t][u];
#pragma unroll
    for (int u = 0; u < 2; ++u)
#pragma unroll
      for (int c = 0; c < 2; ++c) {
        const int a = ra + u * 32 * 128 + c * 64;
        u32x2 X = trd(a), Y = trd(a + 32), X2 = trd(a + 8 * 128), Y2 = trd(a + 8 * 128 + 32);
        W[c][2 * t + u] = u32x8{X.x, Y.x, X.y, Y.y, X2.x, Y2.x, X2.y, Y2.y};
      }
    asm volatile("s_waitcnt lgkmcnt(0)" ::: "memory");
  }
}

#define GLDS16(gptr, ldsaddr) do { u32 keep_; asm volatile("s_mov_b32 %0, m0\n\ts_mov_b32 m0, %2\n\ts_nop 0\n\tglobal_load_lds_dwordx4 %1, off sc1\n\ts_mov_b32 m0, %0" : "=&s"(keep_) : "v"(gptr), "s"(ldsaddr) : "memory"); } while (0)
#define SB0() __builtin_amdgcn_sched_barrier(0)
DEV float fmx(float a, float b) { return __builtin_elementwise_maximum(a, b); }
DEV float xmx32(float a) { auto r = __builtin_amdgcn_permlane32_swap(fu(a), fu(a), false, false); return fmx(uf(r[0]), uf(r[1])); }
DEV float xmx16(float a) { auto r = __builtin_amdgcn_permlane16_swap(fu(a), fu(a), false, false); return fmx(uf(r[0]), uf(r[1])); }
DEV void set_abort(u32* ctrl) { __hip_atomic_store((GAS u32*)ctrl + C_ABORT * 2, 1u, __ATOMIC_RELAXED, __HIP_MEMORY_SCOPE_AGENT); }
constexpr float KSCALE = 0.08838834764831845f * 1.4426950408889634f;

#define ASM_X \
    "ds_read_b128 v[212:215], v11 offset:0\n\t" \
    "ds_read_b128 v[216:219], v11 offset:1024\n\t" \
    "ds_read_b128 v[180:183], v11 offset:20480\n\t" \
    "ds_read_b128 v[184:187], v11 offset:21504\n\t" \
    "ds_read_b128 v[188:191], v11 offset:28672\n\t" \
    "ds_read_b128 v[192:195], v11 offset:29696\n\t" \
    "ds_read_b128 v[196:199], v11 offset:36864\n\t" \
    "ds_read_b128 v[200:203], v11 offset:37888\n\t" \
    "ds_read_b128 v[204:207], v11 offset:45056\n\t" \
    "ds_read_b128 v[208:211], v11 offset:46080\n\t" \
    "v_mov_b64 v[244:245], 0\n\t" \
    "v_mov_b64 v[246:247], 0\n\t" \
    "v_mov_b64 v[248:249], 0\n\t" \
    "v_mov_b64 v[250:251], 0\n\t" \
    "v_mov_b32 v19, 0\n\t" \
    "s_mov_b32 s60, s48\n\t" \
    "s_and_b32 s60, s60, 3\n\t" \
    "s_lshl_b32 s61, s60, 16\n\t" \
    "s_add_u32 s52, s40, s61\n\t" \
    "s_addc_u32 s53, s41, 0\n\t" \
    "s_add_u32 s54, s46, s61\n\t" \
    "s_addc_u32 s55, s47, 0\n\t" \
    "s_lshl_b32 s61, s60, 10\n\t" \
    "s_add_u32 s66, s42, s61\n\t" \
    "s_addc_u32 s67, s43, 0\n\t" \
    "s_add_u32 s61, s60, 0\n\t" \
    "s_lshr_b32 s61, s79, s61\n\t" \
    "s_and_b32 s61, s61, 1\n\t" \
    "s_lshl_b32 s61, s61, 13\n\t" \
    "s_lshl_b32 s77, s60, 19\n\t" \
    "s_or_b32 s61, s61, s77\n\t" \
    "s_add_u32 s56, s44, s61\n\t" \
    "s_addc_u32 s57, s45, 0\n\t" \
    "s_waitcnt lgkmcnt(0)\n\t" \
    "s_memrealtime s[76:77]\n\t" \
    "s_memtime s[28:29]\n\t" \
    "s_waitcnt lgkmcnt(0)\n\t" \
    "s_mul_i32 s70, s76, 24\n\t" \
    "s_mov_b32 s32, s70\n\t" \
    "s_andn2_b32 s70, s70, 15\n\t" \
    "s_mov_b32 s33, s28\n\t" \
    "s_mov_b32 s30, s76\n\t" \
    "90:\n\t" \
    "v_smfmac_f32_16x16x64_bf16 v[244:247], v[212:215], v[20:27], v2\n\t" \
    "v_smfmac_f32_16x16x64_bf16 v[248:251], v[212:215], v[28:35], v2\n\t" \
    "s_waitcnt lgkmcnt(0)\n\t" \
    "s_barrier\n\t" \
    "ds_read_b128 v[220:223], v4 offset:0\n\t" \
    "ds_read_b128 v[224:227], v4 offset:1024\n\t" \
    "ds_read_b128 v[228:231], v5 offset:0\n\t" \
    "ds_read_b128 v[232:235], v5 offset:1024\n\t" \
    "ds_read_b128 v[236:239], v9 offset:0\n\t" \
    "ds_read_b128 v[240:243], v9 offset:1024\n\t" \
    "v_smfmac_f32_16x16x64_bf16 v[244:247], v[216:219], v[36:43], v2\n\t" \
    "v_smfmac_f32_16x16x64_bf16 v[248:251], v[216:219], v[44:51], v2\n\t" \
    "ds_read_b128 v[212:215], v14 offset:0\n\t" \
    "ds_read_b128 v[216:219], v14 offset:4096\n\t" \
    "s_waitcnt lgkmcnt(7)\n\t" \
    "v_smfmac_f32_16x16x64_bf16 v[244:247], v[220:223], v[52:59], v2\n\t" \
    "v_smfmac_f32_16x16x64_bf16 v[248:251], v[220:223], v[60:67], v2\n\t" \
    "s_waitcnt lgkmcnt(6)\n\t" \
    "v_smfmac_f32_16x16x64_bf16 v[244:247], v[224:227], v[68:75], v2\n\t" \
    "v_smfmac_f32_16x16x64_bf16 v[248:251], v[224:227], v[76:83], v2\n\t" \
    "s_waitcnt lgkmcnt(5)\n\t" \
    "v_smfmac_f32_16x16x64_bf16 v[244:247], v[228:231], v[84:91], v2\n\t" \
    "ds_read_b128 v[220:223], v14 offset:1024\n\t" \
    "s_waitcnt lgkmcnt(5)\n\t" \
    "v_smfmac_f32_16x16x64_bf16 v[244:247], v[232:235], v[100:107], v2\n\t" \
    "ds_read_b128 v[224:227], v14 offset:5120\n\t" \
    "s_waitcnt lgkmcnt(5)\n\t" \
    "v_smfmac_f32_16x16x64_bf16 v[244:247], v[236:239], v[180:187], v2\n\t" \
    "s_waitcnt lgkmcnt(4)\n\t" \
    "v_smfmac_f32_16x16x64_bf16 v[244:247], v[240:243], v[196:203], v2\n\t" \
    "ds_read_b128 v[180:183], v14 offset:3072\n\t" \
    "ds_read_b128 v[184:187], v14 offset:7168\n\t" \
    "v_smfmac_f32_16x16x64_bf16 v[248:251], v[228:231], v[92:99], v2\n\t" \
    "ds_read_b128 v[196:199], v15 offset:0\n\t" \
    "ds_read_b128 v[200:203], v15 offset:1024\n\t" \
    "v_smfmac_f32_16x16x64_bf16 v[248:251], v[232:235], v[108:115], v2\n\t" \
    "ds_read_b128 v[228:231], v14 offset:2048\n\t" \
    "v_smfmac_f32_16x16x64_bf16 v[248:251], v[236:239], v[188:195], v2\n\t" \
    "ds_read_b128 v[232:235], v14 offset:6144\n\t" \
    "v_permlane32_swap_b32 v244, v245\n\t" \
    "v_permlane32_swap_b32 v246, v247\n\t" \
    "v_smfmac_f32_16x16x64_bf16 v[248:251], v[240:243], v[204:211], v2\n\t" \
    "v_add_f32 v244, v244, v245\n\t" \
    "v_add_f32 v246, v246, v247\n\t" \
    "v_cvt_pk_bf16_f32 v244, v244, v246\n\t" \
    "ds_write_b16 v6, v244 offset:0\n\t" \
    "ds_write_b16_d16_hi v6, v244 offset:544\n\t" \
    "s_nop 2\n\t" \
    "v_permlane32_swap_b32 v248, v249\n\t" \
    "v_permlane32_swap_b32 v250, v251\n\t" \
    "v_add_f32 v248, v248, v249\n\t" \
    "v_add_f32 v250, v250, v251\n\t" \
    "v_cvt_pk_bf16_f32 v248, v248, v250\n\t" \
    "ds_write_b16 v6, v248 offset:8\n\t" \
    "ds_write_b16_d16_hi v6, v248 offset:552\n\t" \
    "v_mov_b64 v[244:245], 0\n\t" \
    "v_mov_b64 v[246:247], 0\n\t" \
    "v_mov_b64 v[248:249], 0\n\t" \
    "v_mov_b64 v[250:251], 0\n\t" \
    "s_waitcnt lgkmcnt(0)\n\t" \
    "s_barrier\n\t" \
    "ds_read_b128 v[188:191], v7 offset:8192\n\t" \
    "ds_read_b128 v[192:195], v7 offset:8256\n\t" \
    "ds_read_b128 v[204:207], v7 offset:8320\n\t" \
    "ds_read_b128 v[208:211], v7 offset:8384\n\t" \
    "s_waitcnt lgkmcnt(3)\n\t" \
    "v_mfma_f32_16x16x32_bf16 v[236:239], v[212:215], v[188:191], 0\n\t" \
    "v_mfma_f32_16x16x32_bf16 v[240:243], v[216:219], v[188:191], 0\n\t" \
    "s_waitcnt lgkmcnt(2)\n\t" \
    "v_mfma_f32_16x16x32_bf16 v[236:239], v[220:223], v[192:195], v[236:239]\n\t" \
    "v_mfma_f32_16x16x32_bf16 v[240:243], v[224:227], v[192:195], v[240:243]\n\t" \
    "s_waitcnt lgkmcnt(1)\n\t" \
    "v_mfma_f32_16x16x32_bf16 v[236:239], v[228:231], v[204:207], v[236:239]\n\t" \
    "v_mfma_f32_16x16x32_bf16 v[240:243], v[232:235], v[204:207], v[240:243]\n\t" \
    "s_waitcnt lgkmcnt(0)\n\t" \
    "v_mfma_f32_16x16x32_bf16 v[236:239], v[180:183], v[208:211], v[236:239]\n\t" \
    "v_mfma_f32_16x16x32_bf16 v[240:243], v[184:187], v[208:211], v[240:243]\n\t" \
    "v_mov_b32 v224, 0x3f803f80\n\t" \
    "v_mov_b32 v225, 0x3f803f80\n\t" \
    "v_mov_b32 v226, 0x3f803f80\n\t" \
    "v_mov_b32 v227, 0x3f803f80\n\t" \
    "s_nop 2\n\t" \
    "v_exp_f32 v212, v236\n\t" \
    "v_exp_f32 v213, v237\n\t" \
    "v_exp_f32 v214, v238\n\t" \
    "v_exp_f32 v215, v239\n\t" \
    "v_exp_f32 v216, v240\n\t" \
    "v_exp_f32 v217, v241\n\t" \
    "v_exp_f32 v218, v242\n\t" \
    "v_exp_f32 v219, v243\n\t" \
    "v_cvt_pk_bf16_f32 v220, v212, v213\n\t" \
    "v_cvt_pk_bf16_f32 v221, v214, v215\n\t" \
    "v_cvt_pk_bf16_f32 v222, v216, v217\n\t" \
    "v_cvt_pk_bf16_f32 v223, v218, v219\n\t" \
    "s_nop 1\n\t" \
    "v_mfma_f32_16x16x32_bf16 v[188:191], v[196:199], v[220:223], 0\n\t" \
    "v_mfma_f32_16x16x32_bf16 v[192:195], v[200:203], v[220:223], 0\n\t" \
    "v_mfma_f32_16x16x32_bf16 v[204:207], v[224:227], v[220:223], 0\n\t" \
    "v_max3_f32 v17, |v236|, |v237|, |v238|\n\t" \
    "v_max3_f32 v18, |v239|, |v240|, |v241|\n\t" \
    "v_max3_f32 v17, v17, |v242|, |v243|\n\t" \
    "v_max_f32 v17, v17, v18\n\t" \
    "v_cmp_lt_f32 vcc, s62, v17\n\t" \
    "s_nop 0\n\t" \
    "91:\n\t" \
    "v_cvt_pk_bf16_f32 v208, v188, v189\n\t" \
    "v_cvt_pk_bf16_f32 v209, v190, v191\n\t" \
    "v_cvt_pk_bf16_f32 v210, v192, v193\n\t" \
    "v_cvt_pk_bf16_f32 v211, v194, v195\n\t" \
    "s_cbranch_vccnz 95f\n\t" \
    "ds_write_b128 v8, v[208:211]\n\t" \
    "v_smfmac_f32_16x16x64_bf16 v[244:247], v[208:211], v[116:123], v2\n\t" \
    "v_min_u32 v205, -2, v204\n\t" \
    "v_min_u32 v204, -2, v19\n\t" \
    "v_smfmac_f32_16x16x64_bf16 v[248:251], v[208:211], v[148:155], v2\n\t" \
    "s_mov_b64 exec, 0xff\n\t" \
    "global_store_dwordx2 v10, v[204:205], s[54:55] offset:192 sc1\n\t" \
    "s_mov_b64 exec, -1\n\t" \
    "s_waitcnt lgkmcnt(0)\n\t" \
    "s_barrier\n\t" \
    "ds_read_b128 v[212:215], v3 offset:12800\n\t" \
    "ds_read_b128 v[216:219], v12 offset:12800\n\t" \
    "ds_read_b128 v[220:223], v16 offset:12800\n\t" \
    "s_waitcnt lgkmcnt(2)\n\t" \
    "v_smfmac_f32_16x16x64_bf16 v[244:247], v[212:215], v[124:131], v2\n\t" \
    "s_waitcnt lgkmcnt(1)\n\t" \
    "v_smfmac_f32_16x16x64_bf16 v[244:247], v[216:219], v[132:139], v2\n\t" \
    "s_waitcnt lgkmcnt(0)\n\t" \
    "v_smfmac_f32_16x16x64_bf16 v[244:247], v[220:223], v[140:147], v2\n\t" \
    "v_smfmac_f32_16x16x64_bf16 v[248:251], v[212:215], v[156:163], v2\n\t" \
    "v_smfmac_f32_16x16x64_bf16 v[248:251], v[216:219], v[164:171], v2\n\t" \
    "v_smfmac_f32_16x16x64_bf16 v[248:251], v[220:223], v[172:179], v2\n\t" \
    "s_add_u32 s48, s48, 1\n\t" \
    "s_nop 0\n\t" \
    "v_cvt_pk_bf16_f32 v228, v244, v245\n\t" \
    "v_cvt_pk_bf16_f32 v229, v246, v247\n\t" \
    "s_nop 3\n\t" \
    "v_cvt_pk_bf16_f32 v230, v248, v249\n\t" \
    "v_cvt_pk_bf16_f32 v231, v250, v251\n\t" \
    "s_nop 0\n\t" \
    "v_permlane16_swap_b32 v228, v230\n\t" \
    "v_permlane16_swap_b32 v229, v231\n\t" \
    "v_min_u32 v228, -2, v228\n\t" \
    "v_min_u32 v230, -2, v230\n\t" \
    "v_min_u32 v229, -2, v229\n\t" \
    "v_min_u32 v231, -2, v231\n\t" \
    "global_store_dwordx4 v13, v[228:231], s[56:57] sc1\n\t" \
    "s_cmp_eq_u32 s48, 63\n\t" \
    "s_cbranch_scc1 99f\n\t" \
    "v_xor_b32 v14, 0x4000, v14\n\t" \
    "v_xor_b32 v15, 0x4000, v15\n\t" \
    "ds_read_b128 v[180:183], v11 offset:20480\n\t" \
    "ds_read_b128 v[184:187], v11 offset:21504\n\t" \
    "ds_read_b128 v[188:191], v11 offset:28672\n\t" \
    "ds_read_b128 v[192:195], v11 offset:29696\n\t" \
    "ds_read_b128 v[196:199], v11 offset:36864\n\t" \
    "ds_read_b128 v[200:203], v11 offset:37888\n\t" \
    "ds_read_b128 v[204:207], v11 offset:45056\n\t" \
    "ds_read_b128 v[208:211], v11 offset:46080\n\t" \
    "v_mov_b64 v[244:245], 0\n\t" \
    "v_mov_b64 v[246:247], 0\n\t" \
    "v_mov_b64 v[248:249], 0\n\t" \
    "v_mov_b64 v[250:251], 0\n\t" \
    "v_mov_b32 v19, 0\n\t" \
    "s_mov_b32 s60, s48\n\t" \
    "s_and_b32 s60, s60, 3\n\t" \
    "s_lshl_b32 s61, s60, 16\n\t" \
    "s_add_u32 s52, s40, s61\n\t" \
    "s_addc_u32 s53, s41, 0\n\t" \
    "s_add_u32 s54, s46, s61\n\t" \
    "s_addc_u32 s55, s47, 0\n\t" \
    "s_lshl_b32 s61, s60, 10\n\t" \
    "s_add_u32 s66, s42, s61\n\t" \
    "s_addc_u32 s67, s43, 0\n\t" \
    "s_add_u32 s61, s60, 0\n\t" \
    "s_lshr_b32 s61, s79, s61\n\t" \
    "s_and_b32 s61, s61, 1\n\t" \
    "s_lshl_b32 s61, s61, 13\n\t" \
    "s_lshl_b32 s77, s60, 19\n\t" \
    "s_or_b32 s61, s61, s77\n\t" \
    "s_add_u32 s56, s44, s61\n\t" \
    "s_addc_u32 s57, s45, 0\n\t" \
    "s_waitcnt lgkmcnt(0)\n\t" \
    "s_and_b32 s60, s70, 15\n\t" \
    "s_mul_i32 s60, s60, 48\n\t" \
    "s_add_u32 s70, s70, s60\n\t" \
    "s_memrealtime s[76:77]\n\t" \
    "s_memtime s[28:29]\n\t" \
    "s_add_u32 s63, s70, 4704\n\t" \
    "s_waitcnt lgkmcnt(0)\n\t" \
    "s_mul_i32 s61, s76, 24\n\t" \
    "s_sub_u32 s33, s28, s33\n\t" \
    "s_add_u32 s32, s32, s33\n\t" \
    "s_sub_u32 s32, s32, s61\n\t" \
    "s_max_i32 s32, s32, 0\n\t" \
    "s_min_i32 s32, s32, 95\n\t" \
    "s_add_u32 s32, s32, s61\n\t" \
    "s_mov_b32 s33, s28\n\t" \
    "s_branch 103f\n\t" \
    "101:\n\t" \
    "s_memrealtime s[76:77]\n\t" \
    "s_memtime s[28:29]\n\t" \
    "s_waitcnt lgkmcnt(0)\n\t" \
    "s_mul_i32 s61, s76, 24\n\t" \
    "s_cmp_lg_u32 s76, s30\n\t" \
    "s_cselect_b32 s32, s61, s32\n\t" \
    "s_cselect_b32 s33, s28, s33\n\t" \
    "103:\n\t" \
    "s_mov_b32 s30, s76\n\t" \
    "s_sub_u32 s61, s63, s61\n\t" \
    "s_cmp_gt_i32 s61, 330\n\t" \
    "s_cbranch_scc1 101b\n\t" \
    "s_sub_u32 s61, s63, s32\n\t" \
    "s_add_u32 s61, s61, s33\n\t" \
    "s_sub_u32 s61, s61, s28\n\t" \
    "102:\n\t" \
    "s_sub_u32 s61, s61, 34\n\t" \
    "s_cmp_gt_i32 s61, 100\n\t" \
    "s_cbranch_scc1 102b\n\t" \
    "104:\n\t" \
    "global_load_dwordx4 v[212:215], v0, s[52:53] sc1\n\t" \
    "global_load_dwordx4 v[216:219], v0, s[52:53] offset:1024 sc1\n\t" \
    "global_load_dword v220, v1, s[66:67] sc1\n\t" \
    "s_sub_u32 s50, s50, 1\n\t" \
    "s_cselect_b32 s50, 0, s50\n\t" \
    "s_cselect_b64 s[74:75], 0, -1\n\t" \
    "s_waitcnt vmcnt(0)\n\t" \
    "ds_write_b128 v11, v[212:215]\n\t" \
    "v_max3_u32 v17, v212, v213, v214\n\t" \
    "v_max3_u32 v18, v215, v216, v217\n\t" \
    "v_max3_u32 v17, v17, v218, v219\n\t" \
    "v_max3_u32 v17, v17, v18, v220\n\t" \
    "v_cmp_eq_u32 vcc, -1, v17\n\t" \
    "ds_write_b128 v11, v[216:219] offset:1024\n\t" \
    "v_readfirstlane_b32 s70, v220\n\t" \
    "s_and_b64 vcc, vcc, s[74:75]\n\t" \
    "s_cbranch_vccnz 105f\n\t" \
    "s_branch 90b\n\t" \
    "95:\n\t" \
    "v_max3_f32 v17, v236, v237, v238\n\t" \
    "v_max3_f32 v18, v239, v240, v241\n\t" \
    "v_max3_f32 v17, v17, v242, v243\n\t" \
    "v_max_f32 v19, v17, v18\n\t" \
    "v_mov_b32 v17, v19\n\t" \
    "s_nop 1\n\t" \
    "v_permlane16_swap_b32 v19, v17\n\t" \
    "v_max_f32 v19, v19, v17\n\t" \
    "v_mov_b32 v17, v19\n\t" \
    "s_nop 1\n\t" \
    "v_permlane32_swap_b32 v19, v17\n\t" \
    "v_max_f32 v19, v19, v17\n\t" \
    "s_nop 7\n\t" \
    "v_sub_f32 v212, v236, v19\n\t" \
    "v_sub_f32 v213, v237, v19\n\t" \
    "v_sub_f32 v214, v238, v19\n\t" \
    "v_sub_f32 v215, v239, v19\n\t" \
    "v_sub_f32 v216, v240, v19\n\t" \
    "v_sub_f32 v217, v241, v19\n\t" \
    "v_sub_f32 v218, v242, v19\n\t" \
    "v_sub_f32 v219, v243, v19\n\t" \
    "v_exp_f32 v212, v212\n\t" \
    "v_exp_f32 v213, v213\n\t" \
    "v_exp_f32 v214, v214\n\t" \
    "v_exp_f32 v215, v215\n\t" \
    "v_exp_f32 v216, v216\n\t" \
    "v_exp_f32 v217, v217\n\t" \
    "v_exp_f32 v218, v218\n\t" \
    "v_exp_f32 v219, v219\n\t" \
    "v_cvt_pk_bf16_f32 v220, v212, v213\n\t" \
    "v_cvt_pk_bf16_f32 v221, v214, v215\n\t" \
    "v_cvt_pk_bf16_f32 v222, v216, v217\n\t" \
    "v_cvt_pk_bf16_f32 v223, v218, v219\n\t" \
    "s_nop 1\n\t" \
    "v_mfma_f32_16x16x32_bf16 v[188:191], v[196:199], v[220:223], 0\n\t" \
    "v_mfma_f32_16x16x32_bf16 v[192:195], v[200:203], v[220:223], 0\n\t" \
    "v_mfma_f32_16x16x32_bf16 v[204:207], v[224:227], v[220:223], 0\n\t" \
    "s_mov_b64 vcc, 0\n\t" \
    "s_nop 7\n\t" \
    "s_branch 91b\n\t" \
    "105:\n\t" \
    "s_branch 104b\n\t" \
    "99:\n\t" \
    "s_waitcnt vmcnt(0) lgkmcnt(0)\n\t"

#define ASM_M \
    "90:\n\t" \
    "v_mov_b64 v[100:101], 0\n\t" \
    "v_mov_b64 v[102:103], 0\n\t" \
    "v_mov_b64 v[104:105], 0\n\t" \
    "v_mov_b64 v[106:107], 0\n\t" \
    "s_mov_b32 s60, s48\n\t" \
    "s_and_b32 s60, s60, 3\n\t" \
    "s_add_u32 s61, s60, 0\n\t" \
    "s_lshr_b32 s61, s79, s61\n\t" \
    "s_and_b32 s61, s61, 1\n\t" \
    "s_lshl_b32 s61, s61, 13\n\t" \
    "s_lshl_b32 s77, s60, 19\n\t" \
    "s_or_b32 s61, s61, s77\n\t" \
    "s_add_u32 s56, s44, s61\n\t" \
    "s_addc_u32 s57, s45, 0\n\t" \
    "s_barrier\n\t" \
    "89:\n\t" \
    "s_barrier\n\t" \
    "s_barrier\n\t" \
    "ds_read_b128 v[84:87], v10 offset:12800\n\t" \
    "ds_read_b128 v[88:91], v10 offset:13312\n\t" \
    "ds_read_b128 v[92:95], v10 offset:13824\n\t" \
    "ds_read_b128 v[96:99], v10 offset:14336\n\t" \
    "s_waitcnt lgkmcnt(3)\n\t" \
    "v_smfmac_f32_16x16x64_bf16 v[100:103], v[84:87], v[20:27], v2\n\t" \
    "v_smfmac_f32_16x16x64_bf16 v[104:107], v[84:87], v[52:59], v2\n\t" \
    "s_waitcnt lgkmcnt(2)\n\t" \
    "v_smfmac_f32_16x16x64_bf16 v[100:103], v[88:91], v[28:35], v2\n\t" \
    "s_waitcnt lgkmcnt(1)\n\t" \
    "v_smfmac_f32_16x16x64_bf16 v[100:103], v[92:95], v[36:43], v2\n\t" \
    "s_waitcnt lgkmcnt(0)\n\t" \
    "v_smfmac_f32_16x16x64_bf16 v[100:103], v[96:99], v[44:51], v2\n\t" \
    "v_smfmac_f32_16x16x64_bf16 v[104:107], v[88:91], v[60:67], v2\n\t" \
    "v_smfmac_f32_16x16x64_bf16 v[104:107], v[92:95], v[68:75], v2\n\t" \
    "v_smfmac_f32_16x16x64_bf16 v[104:107], v[96:99], v[76:83], v2\n\t" \
    "v_cvt_pk_bf16_f32 v108, v100, v101\n\t" \
    "v_cvt_pk_bf16_f32 v109, v102, v103\n\t" \
    "s_nop 5\n\t" \
    "v_cvt_pk_bf16_f32 v110, v104, v105\n\t" \
    "v_cvt_pk_bf16_f32 v111, v106, v107\n\t" \
    "s_nop 0\n\t" \
    "v_permlane16_swap_b32 v108, v110\n\t" \
    "v_permlane16_swap_b32 v109, v111\n\t" \
    "v_min_u32 v108, -2, v108\n\t" \
    "v_min_u32 v110, -2, v110\n\t" \
    "v_min_u32 v109, -2, v109\n\t" \
    "v_min_u32 v111, -2, v111\n\t" \
    "global_store_dwordx4 v3, v[108:111], s[56:57] sc1\n\t" \
    "s_add_u32 s48, s48, 1\n\t" \
    "s_cmp_eq_u32 s48, 63\n\t" \
    "s_cbranch_scc1 98f\n\t" \
    "s_cmp_lt_u32 s48, 15\n\t" \
    "s_cbranch_scc1 80f\n\t" \
    "s_and_b32 s60, s48, 7\n\t" \
    "s_cmp_eq_u32 s60, 7\n\t" \
    "s_cbranch_scc1 82f\n\t" \
    "s_branch 87f\n\t" \
    "80:\n\t" \
    "s_bitcmp0_b32 s48, 0\n\t" \
    "s_cbranch_scc1 87f\n\t" \
    "s_add_u32 s40, s40, 1024\n\t" \
    "s_addc_u32 s41, s41, 0\n\t" \
    "81:\n\t" \
    "global_load_dwordx4 v[12:15], v1, s[40:41] sc1\n\t" \
    "s_sub_u32 s50, s50, 1\n\t" \
    "s_cselect_b32 s50, 0, s50\n\t" \
    "s_waitcnt vmcnt(0)\n\t" \
    "s_cbranch_scc1 87f\n\t" \
    "v_cmp_ne_u64 vcc, s[46:47], v[12:13]\n\t" \
    "v_cmp_ne_u64 s[60:61], s[46:47], v[14:15]\n\t" \
    "s_or_b64 vcc, vcc, s[60:61]\n\t" \
    "s_cbranch_vccnz 81b\n\t" \
    "s_branch 87f\n\t" \
    "82:\n\t" \
    "s_add_u32 s42, s42, 512\n\t" \
    "s_addc_u32 s43, s43, 0\n\t" \
    "86:\n\t" \
    "global_load_dwordx2 v[12:13], v1, s[42:43] sc1\n\t" \
    "s_sub_u32 s50, s50, 1\n\t" \
    "s_cselect_b32 s50, 0, s50\n\t" \
    "s_waitcnt vmcnt(0)\n\t" \
    "s_cbranch_scc1 87f\n\t" \
    "v_cmp_ne_u64 vcc, s[46:47], v[12:13]\n\t" \
    "s_cbranch_vccnz 86b\n\t" \
    "87:\n\t" \
    "s_sleep 6\n\t" \
    "s_xor_b32 s51, s51, 0x4000\n\t" \
    "s_mov_b32 m0, s51\n\t" \
    "s_add_u32 s64, s64, 262144\n\t" \
    "s_addc_u32 s65, s65, 0\n\t" \
    "global_load_lds_dwordx4 v0, s[64:65] offset:0 sc1\n\t" \
    "global_load_lds_dwordx4 v0, s[64:65] offset:1024 sc1\n\t" \
    "global_load_lds_dwordx4 v0, s[64:65] offset:2048 sc1\n\t" \
    "global_load_lds_dwordx4 v0, s[64:65] offset:3072 sc1\n\t" \
    "s_waitcnt vmcnt(0)\n\t" \
    "s_branch 90b\n\t" \
    "98:\n\t" \
    "s_waitcnt vmcnt(0) lgkmcnt(0)\n\t"

#define ASM_K0 \
    "v_mov_b64 v[180:181], 0\n\t" \
    "v_mov_b64 v[182:183], 0\n\t" \
    "s_mov_b32 s60, s48\n\t" \
    "s_and_b32 s60, s60, 3\n\t" \
    "s_lshl_b32 s61, s60, 16\n\t" \
    "s_add_u32 s52, s40, s61\n\t" \
    "s_addc_u32 s53, s41, 0\n\t" \
    "s_add_u32 s54, s46, s61\n\t" \
    "s_addc_u32 s55, s47, 0\n\t" \
    "s_lshl_b32 s61, s60, 10\n\t" \
    "s_add_u32 s66, s42, s61\n\t" \
    "s_addc_u32 s67, s43, 0\n\t" \
    "s_add_u32 s60, s48, 3\n\t" \
    "s_and_b32 s60, s60, 3\n\t" \
    "s_lshl_b32 s61, s60, 16\n\t" \
    "s_add_u32 s68, s46, s61\n\t" \
    "s_addc_u32 s69, s47, 0\n\t" \
    "s_memrealtime s[76:77]\n\t" \
    "s_memtime s[28:29]\n\t" \
    "s_waitcnt lgkmcnt(0)\n\t" \
    "s_mul_i32 s70, s76, 24\n\t" \
    "s_mov_b32 s32, s70\n\t" \
    "s_andn2_b32 s70, s70, 15\n\t" \
    "s_mov_b32 s33, s28\n\t" \
    "s_mov_b32 s30, s76\n\t" \
    "90:\n\t" \
    "s_waitcnt lgkmcnt(0)\n\t" \
    "s_barrier\n\t" \
    "ds_read_b128 v[148:151], v13 offset:0\n\t" \
    "ds_read_b128 v[152:155], v14 offset:0\n\t" \
    "ds_read_b128 v[156:159], v13 offset:1024\n\t" \
    "ds_read_b128 v[160:163], v14 offset:1024\n\t" \
    "ds_read_b128 v[164:167], v13 offset:2048\n\t" \
    "ds_read_b128 v[168:171], v14 offset:2048\n\t" \
    "ds_read_b128 v[172:175], v13 offset:3072\n\t" \
    "ds_read_b128 v[176:179], v14 offset:3072\n\t" \
    "s_waitcnt lgkmcnt(7)\n\t" \
    "v_smfmac_f32_16x16x64_bf16 v[180:183], v[148:151], v[20:27], v2\n\t" \
    "ds_read_b128 v[148:151], v13 offset:4096\n\t" \
    "s_waitcnt lgkmcnt(7)\n\t" \
    "v_smfmac_f32_16x16x64_bf16 v[180:183], v[152:155], v[28:35], v2\n\t" \
    "ds_read_b128 v[152:155], v14 offset:4096\n\t" \
    "s_waitcnt lgkmcnt(7)\n\t" \
    "v_smfmac_f32_16x16x64_bf16 v[180:183], v[156:159], v[36:43], v2\n\t" \
    "ds_read_b128 v[156:159], v13 offset:5120\n\t" \
    "s_waitcnt lgkmcnt(7)\n\t" \
    "v_smfmac_f32_16x16x64_bf16 v[180:183], v[160:163], v[44:51], v2\n\t" \
    "ds_read_b128 v[160:163], v14 offset:5120\n\t" \
    "s_waitcnt lgkmcnt(7)\n\t" \
    "v_smfmac_f32_16x16x64_bf16 v[180:183], v[164:167], v[52:59], v2\n\t" \
    "ds_read_b128 v[164:167], v13 offset:6144\n\t" \
    "s_waitcnt lgkmcnt(7)\n\t" \
    "v_smfmac_f32_16x16x64_bf16 v[180:183], v[168:171], v[60:67], v2\n\t" \
    "ds_read_b128 v[168:171], v14 offset:6144\n\t" \
    "s_waitcnt lgkmcnt(7)\n\t" \
    "v_smfmac_f32_16x16x64_bf16 v[180:183], v[172:175], v[68:75], v2\n\t" \
    "ds_read_b128 v[172:175], v13 offset:7168\n\t" \
    "s_waitcnt lgkmcnt(7)\n\t" \
    "v_smfmac_f32_16x16x64_bf16 v[180:183], v[176:179], v[76:83], v2\n\t" \
    "ds_read_b128 v[176:179], v14 offset:7168\n\t" \
    "s_waitcnt lgkmcnt(7)\n\t" \
    "v_smfmac_f32_16x16x64_bf16 v[180:183], v[148:151], v[84:91], v2\n\t" \
    "s_waitcnt lgkmcnt(6)\n\t" \
    "v_smfmac_f32_16x16x64_bf16 v[180:183], v[152:155], v[92:99], v2\n\t" \
    "s_waitcnt lgkmcnt(5)\n\t" \
    "v_smfmac_f32_16x16x64_bf16 v[180:183], v[156:159], v[100:107], v2\n\t" \
    "s_waitcnt lgkmcnt(4)\n\t" \
    "v_smfmac_f32_16x16x64_bf16 v[180:183], v[160:163], v[108:115], v2\n\t" \
    "s_waitcnt lgkmcnt(3)\n\t" \
    "v_smfmac_f32_16x16x64_bf16 v[180:183], v[164:167], v[116:123], v2\n\t" \
    "s_waitcnt lgkmcnt(2)\n\t" \
    "v_smfmac_f32_16x16x64_bf16 v[180:183], v[168:171], v[124:131], v2\n\t" \
    "s_waitcnt lgkmcnt(1)\n\t" \
    "v_smfmac_f32_16x16x64_bf16 v[180:183], v[172:175], v[132:139], v2\n\t" \
    "s_waitcnt lgkmcnt(0)\n\t" \
    "v_smfmac_f32_16x16x64_bf16 v[180:183], v[176:179], v[140:147], v2\n\t" \
    "s_nop 7\n\t" \
    "v_cvt_pk_bf16_f32 v17, v180, v181\n\t" \
    "v_cvt_pk_bf16_f32 v18, v182, v183\n\t" \
    "ds_write_b16 v4, v17\n\t" \
    "ds_write_b16_d16_hi v4, v17 offset:272\n\t" \
    "ds_write_b16 v4, v18 offset:544\n\t" \
    "ds_write_b16_d16_hi v4, v18 offset:816\n\t" \
    "s_waitcnt lgkmcnt(0)\n\t" \
    "s_barrier\n\t" \
    "s_cmp_eq_u32 s48, 63\n\t" \
    "s_cbranch_scc1 98f\n\t" \
    "ds_read_b128 v[148:151], v7 offset:10496\n\t" \
    "ds_read_b128 v[164:167], v7 offset:8192\n\t" \
    "ds_read_b128 v[152:155], v7 offset:10560\n\t" \
    "ds_read_b128 v[168:171], v7 offset:8256\n\t" \
    "ds_read_b128 v[156:159], v7 offset:10624\n\t" \
    "ds_read_b128 v[172:175], v7 offset:8320\n\t" \
    "ds_read_b128 v[160:163], v7 offset:10688\n\t" \
    "ds_read_b128 v[176:179], v7 offset:8384\n\t" \
    "s_waitcnt lgkmcnt(6)\n\t" \
    "v_mfma_f32_16x16x32_bf16 v[180:183], v[148:151], v[164:167], 0\n\t" \
    "s_waitcnt lgkmcnt(4)\n\t" \
    "v_mfma_f32_16x16x32_bf16 v[180:183], v[152:155], v[168:171], v[180:183]\n\t" \
    "s_waitcnt lgkmcnt(2)\n\t" \
    "v_mfma_f32_16x16x32_bf16 v[180:183], v[156:159], v[172:175], v[180:183]\n\t" \
    "s_waitcnt lgkmcnt(0)\n\t" \
    "v_mfma_f32_16x16x32_bf16 v[180:183], v[160:163], v[176:179], v[180:183]\n\t" \
    "v_mov_b64 v[148:149], 0\n\t" \
    "s_nop 6\n\t" \
    "v_pk_mul_f32 v[180:181], v[180:181], v[10:11]\n\t" \
    "v_pk_mul_f32 v[182:183], v[182:183], v[10:11]\n\t" \
    "v_max3_f32 v17, |v180|, |v181|, |v182|\n\t" \
    "v_max_f32 v17, v17, |v183|\n\t" \
    "v_cmp_lt_f32 vcc, s62, v17\n\t" \
    "v_exp_f32 v184, v180\n\t" \
    "v_exp_f32 v185, v181\n\t" \
    "v_exp_f32 v186, v182\n\t" \
    "v_exp_f32 v187, v183\n\t" \
    "s_cbranch_vccnz 95f\n\t" \
    "91:\n\t" \
    "v_cvt_pk_bf16_f32 v150, v184, v185\n\t" \
    "v_cvt_pk_bf16_f32 v151, v186, v187\n\t" \
    "s_mov_b64 exec, s[58:59]\n\t" \
    "v_min_u32 v150, -2, v150\n\t" \
    "v_min_u32 v151, -2, v151\n\t" \
    "global_store_dwordx2 v9, v[150:151], s[54:55] sc1\n\t" \
    "global_store_dwordx2 v5, v[150:151], s[54:55] sc1\n\t" \
    "global_store_dwordx2 v6, v[148:149], s[54:55] sc1\n\t" \
    "s_mov_b64 exec, -1\n\t" \
    "global_store_dwordx4 v3, v[188:191], s[68:69] sc1\n\t" \
    "s_add_u32 s48, s48, 1\n\t" \
    "v_mov_b64 v[180:181], 0\n\t" \
    "v_mov_b64 v[182:183], 0\n\t" \
    "s_mov_b32 s60, s48\n\t" \
    "s_and_b32 s60, s60, 3\n\t" \
    "s_lshl_b32 s61, s60, 16\n\t" \
    "s_add_u32 s52, s40, s61\n\t" \
    "s_addc_u32 s53, s41, 0\n\t" \
    "s_add_u32 s54, s46, s61\n\t" \
    "s_addc_u32 s55, s47, 0\n\t" \
    "s_lshl_b32 s61, s60, 10\n\t" \
    "s_add_u32 s66, s42, s61\n\t" \
    "s_addc_u32 s67, s43, 0\n\t" \
    "s_add_u32 s60, s48, 3\n\t" \
    "s_and_b32 s60, s60, 3\n\t" \
    "s_lshl_b32 s61, s60, 16\n\t" \
    "s_add_u32 s68, s46, s61\n\t" \
    "s_addc_u32 s69, s47, 0\n\t" \
    "s_and_b32 s60, s70, 15\n\t" \
    "s_mul_i32 s60, s60, 48\n\t" \
    "s_add_u32 s70, s70, s60\n\t" \
    "s_memrealtime s[76:77]\n\t" \
    "s_memtime s[28:29]\n\t" \
    "s_add_u32 s63, s70, 4704\n\t" \
    "s_waitcnt lgkmcnt(0)\n\t" \
    "s_mul_i32 s61, s76, 24\n\t" \
    "s_sub_u32 s33, s28, s33\n\t" \
    "s_add_u32 s32, s32, s33\n\t" \
    "s_sub_u32 s32, s32, s61\n\t" \
    "s_max_i32 s32, s32, 0\n\t" \
    "s_min_i32 s32, s32, 95\n\t" \
    "s_add_u32 s32, s32, s61\n\t" \
    "s_mov_b32 s33, s28\n\t" \
    "s_branch 108f\n\t" \
    "106:\n\t" \
    "s_memrealtime s[76:77]\n\t" \
    "s_memtime s[28:29]\n\t" \
    "s_waitcnt lgkmcnt(0)\n\t" \
    "s_mul_i32 s61, s76, 24\n\t" \
    "s_cmp_lg_u32 s76, s30\n\t" \
    "s_cselect_b32 s32, s61, s32\n\t" \
    "s_cselect_b32 s33, s28, s33\n\t" \
    "108:\n\t" \
    "s_mov_b32 s30, s76\n\t" \
    "s_sub_u32 s61, s63, s61\n\t" \
    "s_cmp_gt_i32 s61, 330\n\t" \
    "s_cbranch_scc1 106b\n\t" \
    "s_sub_u32 s61, s63, s32\n\t" \
    "s_add_u32 s61, s61, s33\n\t" \
    "s_sub_u32 s61, s61, s28\n\t" \
    "107:\n\t" \
    "s_sub_u32 s61, s61, 34\n\t" \
    "s_cmp_gt_i32 s61, 100\n\t" \
    "s_cbranch_scc1 107b\n\t" \
    "109:\n\t" \
    "global_load_dwordx4 v[148:151], v0, s[52:53] sc1\n\t" \
    "global_load_dwordx4 v[152:155], v0, s[52:53] offset:1024 sc1\n\t" \
    "global_load_dword v16, v1, s[66:67] sc1\n\t" \
    "s_sub_u32 s50, s50, 1\n\t" \
    "s_cselect_b32 s50, 0, s50\n\t" \
    "s_cselect_b64 s[74:75], 0, -1\n\t" \
    "s_waitcnt vmcnt(0)\n\t" \
    "ds_write_b128 v12, v[148:151]\n\t" \
    "v_max3_u32 v17, v148, v149, v150\n\t" \
    "v_max3_u32 v18, v151, v152, v153\n\t" \
    "v_max3_u32 v17, v17, v154, v155\n\t" \
    "v_max3_u32 v17, v17, v18, v16\n\t" \
    "v_cmp_eq_u32 vcc, -1, v17\n\t" \
    "ds_write_b128 v12, v[152:155] offset:1024\n\t" \
    "v_readfirstlane_b32 s70, v16\n\t" \
    "s_and_b64 vcc, vcc, s[74:75]\n\t" \
    "s_cbranch_vccnz 110f\n\t" \
    "s_branch 90b\n\t" \
    "95:\n\t" \
    "v_max3_f32 v148, v180, v181, v182\n\t" \
    "v_max_f32 v148, v148, v183\n\t" \
    "v_mov_b32 v17, v148\n\t" \
    "s_nop 1\n\t" \
    "v_permlane16_swap_b32 v148, v17\n\t" \
    "v_max_f32 v148, v148, v17\n\t" \
    "v_sub_f32 v184, v180, v148\n\t" \
    "v_sub_f32 v185, v181, v148\n\t" \
    "v_sub_f32 v186, v182, v148\n\t" \
    "v_sub_f32 v187, v183, v148\n\t" \
    "v_exp_f32 v184, v184\n\t" \
    "v_exp_f32 v185, v185\n\t" \
    "v_exp_f32 v186, v186\n\t" \
    "v_exp_f32 v187, v187\n\t" \
    "v_min_u32 v148, -2, v148\n\t" \
    "s_branch 91b\n\t" \
    "110:\n\t" \
    "s_branch 109b\n\t" \
    "98:\n\t" \
    "s_waitcnt vmcnt(0) lgkmcnt(0)\n\t"

#define ASM_K1 \
    "v_mov_b64 v[180:181], 0\n\t" \
    "v_mov_b64 v[182:183], 0\n\t" \
    "s_mov_b32 s60, s48\n\t" \
    "s_and_b32 s60, s60, 3\n\t" \
    "s_lshl_b32 s61, s60, 16\n\t" \
    "s_add_u32 s52, s40, s61\n\t" \
    "s_addc_u32 s53, s41, 0\n\t" \
    "s_add_u32 s54, s46, s61\n\t" \
    "s_addc_u32 s55, s47, 0\n\t" \
    "s_lshl_b32 s61, s60, 10\n\t" \
    "s_add_u32 s66, s42, s61\n\t" \
    "s_addc_u32 s67, s43, 0\n\t" \
    "s_memrealtime s[76:77]\n\t" \
    "s_memtime s[28:29]\n\t" \
    "s_waitcnt lgkmcnt(0)\n\t" \
    "s_mul_i32 s70, s76, 24\n\t" \
    "s_mov_b32 s32, s70\n\t" \
    "s_andn2_b32 s70, s70, 15\n\t" \
    "s_mov_b32 s33, s28\n\t" \
    "s_mov_b32 s30, s76\n\t" \
    "90:\n\t" \
    "s_waitcnt lgkmcnt(0)\n\t" \
    "s_barrier\n\t" \
    "ds_read_b128 v[148:151], v13 offset:0\n\t" \
    "ds_read_b128 v[152:155], v14 offset:0\n\t" \
    "ds_read_b128 v[156:159], v13 offset:1024\n\t" \
    "ds_read_b128 v[160:163], v14 offset:1024\n\t" \
    "ds_read_b128 v[164:167], v13 offset:2048\n\t" \
    "ds_read_b128 v[168:171], v14 offset:2048\n\t" \
    "ds_read_b128 v[172:175], v13 offset:3072\n\t" \
    "ds_read_b128 v[176:179], v14 offset:3072\n\t" \
    "s_waitcnt lgkmcnt(7)\n\t" \
    "v_smfmac_f32_16x16x64_bf16 v[180:183], v[148:151], v[20:27], v2\n\t" \
    "ds_read_b128 v[148:151], v13 offset:4096\n\t" \
    "s_waitcnt lgkmcnt(7)\n\t" \
    "v_smfmac_f32_16x16x64_bf16 v[180:183], v[152:155], v[28:35], v2\n\t" \
    "ds_read_b128 v[152:155], v14 offset:4096\n\t" \
    "s_waitcnt lgkmcnt(7)\n\t" \
    "v_smfmac_f32_16x16x64_bf16 v[180:183], v[156:159], v[36:43], v2\n\t" \
    "ds_read_b128 v[156:159], v13 offset:5120\n\t" \
    "s_waitcnt lgkmcnt(7)\n\t" \
    "v_smfmac_f32_16x16x64_bf16 v[180:183], v[160:163], v[44:51], v2\n\t" \
    "ds_read_b128 v[160:163], v14 offset:5120\n\t" \
    "s_waitcnt lgkmcnt(7)\n\t" \
    "v_smfmac_f32_16x16x64_bf16 v[180:183], v[164:167], v[52:59], v2\n\t" \
    "ds_read_b128 v[164:167], v13 offset:6144\n\t" \
    "s_waitcnt lgkmcnt(7)\n\t" \
    "v_smfmac_f32_16x16x64_bf16 v[180:183], v[168:171], v[60:67], v2\n\t" \
    "ds_read_b128 v[168:171], v14 offset:6144\n\t" \
    "s_waitcnt lgkmcnt(7)\n\t" \
    "v_smfmac_f32_16x16x64_bf16 v[180:183], v[172:175], v[68:75], v2\n\t" \
    "ds_read_b128 v[172:175], v13 offset:7168\n\t" \
    "s_waitcnt lgkmcnt(7)\n\t" \
    "v_smfmac_f32_16x16x64_bf16 v[180:183], v[176:179], v[76:83], v2\n\t" \
    "ds_read_b128 v[176:179], v14 offset:7168\n\t" \
    "s_waitcnt lgkmcnt(7)\n\t" \
    "v_smfmac_f32_16x16x64_bf16 v[180:183], v[148:151], v[84:91], v2\n\t" \
    "s_waitcnt lgkmcnt(6)\n\t" \
    "v_smfmac_f32_16x16x64_bf16 v[180:183], v[152:155], v[92:99], v2\n\t" \
    "s_waitcnt lgkmcnt(5)\n\t" \
    "v_smfmac_f32_16x16x64_bf16 v[180:183], v[156:159], v[100:107], v2\n\t" \
    "s_waitcnt lgkmcnt(4)\n\t" \
    "v_smfmac_f32_16x16x64_bf16 v[180:183], v[160:163], v[108:115], v2\n\t" \
    "s_waitcnt lgkmcnt(3)\n\t" \
    "v_smfmac_f32_16x16x64_bf16 v[180:183], v[164:167], v[116:123], v2\n\t" \
    "s_waitcnt lgkmcnt(2)\n\t" \
    "v_smfmac_f32_16x16x64_bf16 v[180:183], v[168:171], v[124:131], v2\n\t" \
    "s_waitcnt lgkmcnt(1)\n\t" \
    "v_smfmac_f32_16x16x64_bf16 v[180:183], v[172:175], v[132:139], v2\n\t" \
    "s_waitcnt lgkmcnt(0)\n\t" \
    "v_smfmac_f32_16x16x64_bf16 v[180:183], v[176:179], v[140:147], v2\n\t" \
    "s_nop 7\n\t" \
    "v_cvt_pk_bf16_f32 v17, v180, v181\n\t" \
    "v_cvt_pk_bf16_f32 v18, v182, v183\n\t" \
    "ds_write_b16 v4, v17\n\t" \
    "ds_write_b16_d16_hi v4, v17 offset:272\n\t" \
    "ds_write_b16 v4, v18 offset:544\n\t" \
    "ds_write_b16_d16_hi v4, v18 offset:816\n\t" \
    "s_waitcnt lgkmcnt(0)\n\t" \
    "s_barrier\n\t" \
    "s_cmp_eq_u32 s48, 63\n\t" \
    "s_cbranch_scc1 98f\n\t" \
    "s_add_u32 s48, s48, 1\n\t" \
    "v_mov_b64 v[180:181], 0\n\t" \
    "v_mov_b64 v[182:183], 0\n\t" \
    "s_mov_b32 s60, s48\n\t" \
    "s_and_b32 s60, s60, 3\n\t" \
    "s_lshl_b32 s61, s60, 16\n\t" \
    "s_add_u32 s52, s40, s61\n\t" \
    "s_addc_u32 s53, s41, 0\n\t" \
    "s_add_u32 s54, s46, s61\n\t" \
    "s_addc_u32 s55, s47, 0\n\t" \
    "s_lshl_b32 s61, s60, 10\n\t" \
    "s_add_u32 s66, s42, s61\n\t" \
    "s_addc_u32 s67, s43, 0\n\t" \
    "s_and_b32 s60, s70, 15\n\t" \
    "s_mul_i32 s60, s60, 48\n\t" \
    "s_add_u32 s70, s70, s60\n\t" \
    "s_memrealtime s[76:77]\n\t" \
    "s_memtime s[28:29]\n\t" \
    "s_add_u32 s63, s70, 4704\n\t" \
    "s_waitcnt lgkmcnt(0)\n\t" \
    "s_mul_i32 s61, s76, 24\n\t" \
    "s_sub_u32 s33, s28, s33\n\t" \
    "s_add_u32 s32, s32, s33\n\t" \
    "s_sub_u32 s32, s32, s61\n\t" \
    "s_max_i32 s32, s32, 0\n\t" \
    "s_min_i32 s32, s32, 95\n\t" \
    "s_add_u32 s32, s32, s61\n\t" \
    "s_mov_b32 s33, s28\n\t" \
    "s_branch 113f\n\t" \
    "111:\n\t" \
    "s_memrealtime s[76:77]\n\t" \
    "s_memtime s[28:29]\n\t" \
    "s_waitcnt lgkmcnt(0)\n\t" \
    "s_mul_i32 s61, s76, 24\n\t" \
    "s_cmp_lg_u32 s76, s30\n\t" \
    "s_cselect_b32 s32, s61, s32\n\t" \
    "s_cselect_b32 s33, s28, s33\n\t" \
    "113:\n\t" \
    "s_mov_b32 s30, s76\n\t" \
    "s_sub_u32 s61, s63, s61\n\t" \
    "s_cmp_gt_i32 s61, 330\n\t" \
    "s_cbranch_scc1 111b\n\t" \
    "s_sub_u32 s61, s63, s32\n\t" \
    "s_add_u32 s61, s61, s33\n\t" \
    "s_sub_u32 s61, s61, s28\n\t" \
    "112:\n\t" \
    "s_sub_u32 s61, s61, 34\n\t" \
    "s_cmp_gt_i32 s61, 100\n\t" \
    "s_cbranch_scc1 112b\n\t" \
    "114:\n\t" \
    "global_load_dwordx4 v[148:151], v0, s[52:53] sc1\n\t" \
    "global_load_dwordx4 v[152:155], v0, s[52:53] offset:1024 sc1\n\t" \
    "global_load_dword v16, v1, s[66:67] sc1\n\t" \
    "s_sub_u32 s50, s50, 1\n\t" \
    "s_cselect_b32 s50, 0, s50\n\t" \
    "s_cselect_b64 s[74:75], 0, -1\n\t" \
    "s_waitcnt vmcnt(0)\n\t" \
    "ds_write_b128 v12, v[148:151]\n\t" \
    "v_max3_u32 v17, v148, v149, v150\n\t" \
    "v_max3_u32 v18, v151, v152, v153\n\t" \
    "v_max3_u32 v17, v17, v154, v155\n\t" \
    "v_max3_u32 v17, v17, v18, v16\n\t" \
    "v_cmp_eq_u32 vcc, -1, v17\n\t" \
    "ds_write_b128 v12, v[152:155] offset:1024\n\t" \
    "v_readfirstlane_b32 s70, v16\n\t" \
    "s_and_b64 vcc, vcc, s[74:75]\n\t" \
    "s_cbranch_vccnz 115f\n\t" \
    "s_branch 90b\n\t" \
    "115:\n\t" \
    "s_branch 114b\n\t" \
    "98:\n\t" \
    "s_waitcnt vmcnt(0) lgkmcnt(0)\n\t"

#define ASM_K4 \
    "v_mov_b64 v[180:181], 0\n\t" \
    "v_mov_b64 v[182:183], 0\n\t" \
    "90:\n\t" \
    "s_waitcnt lgkmcnt(0)\n\t" \
    "s_barrier\n\t" \
    "ds_read_b128 v[148:151], v13 offset:0\n\t" \
    "ds_read_b128 v[152:155], v14 offset:0\n\t" \
    "ds_read_b128 v[156:159], v13 offset:1024\n\t" \
    "ds_read_b128 v[160:163], v14 offset:1024\n\t" \
    "ds_read_b128 v[164:167], v13 offset:2048\n\t" \
    "ds_read_b128 v[168:171], v14 offset:2048\n\t" \
    "ds_read_b128 v[172:175], v13 offset:3072\n\t" \
    "ds_read_b128 v[176:179], v14 offset:3072\n\t" \
    "s_waitcnt lgkmcnt(7)\n\t" \
    "v_smfmac_f32_16x16x64_bf16 v[180:183], v[148:151], v[20:27], v2\n\t" \
    "ds_read_b128 v[148:151], v13 offset:4096\n\t" \
    "s_waitcnt lgkmcnt(7)\n\t" \
    "v_smfmac_f32_16x16x64_bf16 v[180:183], v[152:155], v[28:35], v2\n\t" \
    "ds_read_b128 v[152:155], v14 offset:4096\n\t" \
    "s_waitcnt lgkmcnt(7)\n\t" \
    "v_smfmac_f32_16x16x64_bf16 v[180:183], v[156:159], v[36:43], v2\n\t" \
    "ds_read_b128 v[156:159], v13 offset:5120\n\t" \
    "s_waitcnt lgkmcnt(7)\n\t" \
    "v_smfmac_f32_16x16x64_bf16 v[180:183], v[160:163], v[44:51], v2\n\t" \
    "ds_read_b128 v[160:163], v14 offset:5120\n\t" \
    "s_waitcnt lgkmcnt(7)\n\t" \
    "v_smfmac_f32_16x16x64_bf16 v[180:183], v[164:167], v[52:59], v2\n\t" \
    "ds_read_b128 v[164:167], v13 offset:6144\n\t" \
    "s_waitcnt lgkmcnt(7)\n\t" \
    "v_smfmac_f32_16x16x64_bf16 v[180:183], v[168:171], v[60:67], v2\n\t" \
    "ds_read_b128 v[168:171], v14 offset:6144\n\t" \
    "s_waitcnt lgkmcnt(7)\n\t" \
    "v_smfmac_f32_16x16x64_bf16 v[180:183], v[172:175], v[68:75], v2\n\t" \
    "ds_read_b128 v[172:175], v13 offset:7168\n\t" \
    "s_waitcnt lgkmcnt(7)\n\t" \
    "v_smfmac_f32_16x16x64_bf16 v[180:183], v[176:179], v[76:83], v2\n\t" \
    "ds_read_b128 v[176:179], v14 offset:7168\n\t" \
    "s_waitcnt lgkmcnt(7)\n\t" \
    "v_smfmac_f32_16x16x64_bf16 v[180:183], v[148:151], v[84:91], v2\n\t" \
    "s_waitcnt lgkmcnt(6)\n\t" \
    "v_smfmac_f32_16x16x64_bf16 v[180:183], v[152:155], v[92:99], v2\n\t" \
    "s_waitcnt lgkmcnt(5)\n\t" \
    "v_smfmac_f32_16x16x64_bf16 v[180:183], v[156:159], v[100:107], v2\n\t" \
    "s_waitcnt lgkmcnt(4)\n\t" \
    "v_smfmac_f32_16x16x64_bf16 v[180:183], v[160:163], v[108:115], v2\n\t" \
    "s_waitcnt lgkmcnt(3)\n\t" \
    "v_smfmac_f32_16x16x64_bf16 v[180:183], v[164:167], v[116:123], v2\n\t" \
    "s_waitcnt lgkmcnt(2)\n\t" \
    "v_smfmac_f32_16x16x64_bf16 v[180:183], v[168:171], v[124:131], v2\n\t" \
    "s_waitcnt lgkmcnt(1)\n\t" \
    "v_smfmac_f32_16x16x64_bf16 v[180:183], v[172:175], v[132:139], v2\n\t" \
    "s_waitcnt lgkmcnt(0)\n\t" \
    "v_smfmac_f32_16x16x64_bf16 v[180:183], v[176:179], v[140:147], v2\n\t" \
    "s_nop 7\n\t" \
    "v_cvt_pk_bf16_f32 v17, v180, v181\n\t" \
    "v_cvt_pk_bf16_f32 v18, v182, v183\n\t" \
    "ds_write_b16 v4, v17\n\t" \
    "ds_write_b16_d16_hi v4, v17 offset:272\n\t" \
    "ds_write_b16 v4, v18 offset:544\n\t" \
    "ds_write_b16_d16_hi v4, v18 offset:816\n\t" \
    "s_waitcnt lgkmcnt(0)\n\t" \
    "s_barrier\n\t" \
    "ds_read_b128 v[148:151], v7 offset:10496\n\t" \
    "ds_read_b128 v[152:155], v7 offset:10624\n\t" \
    "s_waitcnt lgkmcnt(0)\n\t" \
    "global_store_dwordx4 v0, v[148:151], s[64:65] sc1\n\t" \
    "global_store_dwordx4 v0, v[152:155], s[64:65] offset:1024 sc1\n\t" \
    "s_add_u32 s64, s64, 65536\n\t" \
    "s_addc_u32 s65, s65, 0\n\t" \
    "s_waitcnt vmcnt(0)\n\t" \
    "s_mov_b64 exec, 1\n\t" \
    "global_store_dwordx2 v1, v[192:193], s[44:45] sc1\n\t" \
    "s_mov_b64 exec, -1\n\t" \
    "v_add_u32 v192, 1, v192\n\t" \
    "s_cmp_eq_u32 s48, 63\n\t" \
    "s_cbranch_scc1 98f\n\t" \
    "s_add_u32 s48, s48, 1\n\t" \
    "v_mov_b64 v[180:181], 0\n\t" \
    "v_mov_b64 v[182:183], 0\n\t" \
    "s_branch 90b\n\t" \
    "98:\n\t" \
    "s_waitcnt vmcnt(0) lgkmcnt(0)\n\t"

#define ASM_K5 \
    "v_mov_b64 v[180:181], 0\n\t" \
    "v_mov_b64 v[182:183], 0\n\t" \
    "90:\n\t" \
    "s_waitcnt lgkmcnt(0)\n\t" \
    "s_barrier\n\t" \
    "ds_read_b128 v[148:151], v13 offset:0\n\t" \
    "ds_read_b128 v[152:155], v14 offset:0\n\t" \
    "ds_read_b128 v[156:159], v13 offset:1024\n\t" \
    "ds_read_b128 v[160:163], v14 offset:1024\n\t" \
    "ds_read_b128 v[164:167], v13 offset:2048\n\t" \
    "ds_read_b128 v[168:171], v14 offset:2048\n\t" \
    "ds_read_b128 v[172:175], v13 offset:3072\n\t" \
    "ds_read_b128 v[176:179], v14 offset:3072\n\t" \
    "s_waitcnt lgkmcnt(7)\n\t" \
    "v_smfmac_f32_16x16x64_bf16 v[180:183], v[148:151], v[20:27], v2\n\t" \
    "ds_read_b128 v[148:151], v13 offset:4096\n\t" \
    "s_waitcnt lgkmcnt(7)\n\t" \
    "v_smfmac_f32_16x16x64_bf16 v[180:183], v[152:155], v[28:35], v2\n\t" \
    "ds_read_b128 v[152:155], v14 offset:4096\n\t" \
    "s_waitcnt lgkmcnt(7)\n\t" \
    "v_smfmac_f32_16x16x64_bf16 v[180:183], v[156:159], v[36:43], v2\n\t" \
    "ds_read_b128 v[156:159], v13 offset:5120\n\t" \
    "s_waitcnt lgkmcnt(7)\n\t" \
    "v_smfmac_f32_16x16x64_bf16 v[180:183], v[160:163], v[44:51], v2\n\t" \
    "ds_read_b128 v[160:163], v14 offset:5120\n\t" \
    "s_waitcnt lgkmcnt(7)\n\t" \
    "v_smfmac_f32_16x16x64_bf16 v[180:183], v[164:167], v[52:59], v2\n\t" \
    "ds_read_b128 v[164:167], v13 offset:6144\n\t" \
    "s_waitcnt lgkmcnt(7)\n\t" \
    "v_smfmac_f32_16x16x64_bf16 v[180:183], v[168:171], v[60:67], v2\n\t" \
    "ds_read_b128 v[168:171], v14 offset:6144\n\t" \
    "s_waitcnt lgkmcnt(7)\n\t" \
    "v_smfmac_f32_16x16x64_bf16 v[180:183], v[172:175], v[68:75], v2\n\t" \
    "ds_read_b128 v[172:175], v13 offset:7168\n\t" \
    "s_waitcnt lgkmcnt(7)\n\t" \
    "v_smfmac_f32_16x16x64_bf16 v[180:183], v[176:179], v[76:83], v2\n\t" \
    "ds_read_b128 v[176:179], v14 offset:7168\n\t" \
    "s_waitcnt lgkmcnt(7)\n\t" \
    "v_smfmac_f32_16x16x64_bf16 v[180:183], v[148:151], v[84:91], v2\n\t" \
    "s_waitcnt lgkmcnt(6)\n\t" \
    "v_smfmac_f32_16x16x64_bf16 v[180:183], v[152:155], v[92:99], v2\n\t" \
    "s_waitcnt lgkmcnt(5)\n\t" \
    "v_smfmac_f32_16x16x64_bf16 v[180:183], v[156:159], v[100:107], v2\n\t" \
    "s_waitcnt lgkmcnt(4)\n\t" \
    "v_smfmac_f32_16x16x64_bf16 v[180:183], v[160:163], v[108:115], v2\n\t" \
    "s_waitcnt lgkmcnt(3)\n\t" \
    "v_smfmac_f32_16x16x64_bf16 v[180:183], v[164:167], v[116:123], v2\n\t" \
    "s_waitcnt lgkmcnt(2)\n\t" \
    "v_smfmac_f32_16x16x64_bf16 v[180:183], v[168:171], v[124:131], v2\n\t" \
    "s_waitcnt lgkmcnt(1)\n\t" \
    "v_smfmac_f32_16x16x64_bf16 v[180:183], v[172:175], v[132:139], v2\n\t" \
    "s_waitcnt lgkmcnt(0)\n\t" \
    "v_smfmac_f32_16x16x64_bf16 v[180:183], v[176:179], v[140:147], v2\n\t" \
    "s_nop 7\n\t" \
    "v_cvt_pk_bf16_f32 v17, v180, v181\n\t" \
    "v_cvt_pk_bf16_f32 v18, v182, v183\n\t" \
    "ds_write_b16 v4, v17\n\t" \
    "ds_write_b16_d16_hi v4, v17 offset:272\n\t" \
    "ds_write_b16 v4, v18 offset:544\n\t" \
    "ds_write_b16_d16_hi v4, v18 offset:816\n\t" \
    "s_waitcnt lgkmcnt(0)\n\t" \
    "s_barrier\n\t" \
    "s_cmp_eq_u32 s48, 63\n\t" \
    "s_cbranch_scc1 98f\n\t" \
    "s_add_u32 s48, s48, 1\n\t" \
    "v_mov_b64 v[180:181], 0\n\t" \
    "v_mov_b64 v[182:183], 0\n\t" \
    "s_branch 90b\n\t" \
    "98:\n\t" \
    "s_waitcnt vmcnt(0) lgkmcnt(0)\n\t"

#define ASM_H0 \
    "v_mov_b64 v[180:181], 0\n\t" \
    "v_mov_b64 v[182:183], 0\n\t" \
    "v_mov_b64 v[184:185], 0\n\t" \
    "v_mov_b64 v[186:187], 0\n\t" \
    "s_mov_b32 s60, s48\n\t" \
    "s_and_b32 s60, s60, 3\n\t" \
    "s_add_u32 s61, s60, 4\n\t" \
    "s_lshr_b32 s61, s79, s61\n\t" \
    "s_and_b32 s61, s61, 1\n\t" \
    "s_lshl_b32 s61, s61, 13\n\t" \
    "s_lshl_b32 s77, s60, 19\n\t" \
    "s_or_b32 s61, s61, s77\n\t" \
    "s_add_u32 s58, s46, s61\n\t" \
    "s_addc_u32 s59, s47, 0\n\t" \
    "s_add_u32 s61, s60, 0\n\t" \
    "s_lshr_b32 s61, s79, s61\n\t" \
    "s_and_b32 s61, s61, 1\n\t" \
    "s_lshl_b32 s61, s61, 13\n\t" \
    "s_lshl_b32 s77, s60, 19\n\t" \
    "s_or_b32 s61, s61, s77\n\t" \
    "s_add_u32 s56, s44, s61\n\t" \
    "s_addc_u32 s57, s45, 0\n\t" \
    "s_lshl_b32 s61, s60, 16\n\t" \
    "s_add_u32 s54, s42, s61\n\t" \
    "s_addc_u32 s55, s43, 0\n\t" \
    "s_add_u32 s52, s40, s61\n\t" \
    "s_addc_u32 s53, s41, 0\n\t" \
    "s_lshl_b32 s61, s60, 10\n\t" \
    "s_add_u32 s66, s36, s61\n\t" \
    "s_addc_u32 s67, s37, 0\n\t" \
    "ds_read_b128 v[148:151], v11 offset:0\n\t" \
    "ds_read_b128 v[152:155], v11 offset:1024\n\t" \
    "s_memrealtime s[76:77]\n\t" \
    "s_memtime s[28:29]\n\t" \
    "s_waitcnt lgkmcnt(0)\n\t" \
    "s_mul_i32 s70, s76, 24\n\t" \
    "s_mov_b32 s32, s70\n\t" \
    "s_andn2_b32 s70, s70, 15\n\t" \
    "s_mov_b32 s33, s28\n\t" \
    "s_mov_b32 s30, s76\n\t" \
    "s_mov_b32 s68, 0x00ff00ff\n\t" \
    "s_mov_b32 s69, 0x00ff00ff\n\t" \
    "s_mov_b32 s71, 1008\n\t" \
    "s_mov_b32 s49, 1\n\t" \
    "s_mov_b32 s31, 0\n\t" \
    "s_mov_b32 s62, 32\n\t" \
    "s_mov_b32 s98, 0\n\t" \
    "s_mov_b32 s99, 1\n\t" \
    "ds_write_b32 v1, v1 offset:16900\n\t" \
    "90:\n\t" \
    "v_smfmac_f32_16x16x64_bf16 v[180:183], v[148:151], v[20:27], v2\n\t" \
    "v_smfmac_f32_16x16x64_bf16 v[184:187], v[148:151], v[28:35], v2\n\t" \
    "s_waitcnt lgkmcnt(0)\n\t" \
    "s_barrier\n\t" \
    "ds_read_b128 v[156:159], v3 offset:0\n\t" \
    "ds_read_b128 v[160:163], v3 offset:1024\n\t" \
    "ds_read_b128 v[164:167], v5 offset:0\n\t" \
    "ds_read_b128 v[168:171], v5 offset:1024\n\t" \
    "ds_read_b128 v[172:175], v10 offset:0\n\t" \
    "ds_read_b128 v[176:179], v10 offset:1024\n\t" \
    "v_smfmac_f32_16x16x64_bf16 v[180:183], v[152:155], v[36:43], v2\n\t" \
    "v_smfmac_f32_16x16x64_bf16 v[184:187], v[152:155], v[44:51], v2\n\t" \
    "s_waitcnt lgkmcnt(5)\n\t" \
    "v_smfmac_f32_16x16x64_bf16 v[180:183], v[156:159], v[52:59], v2\n\t" \
    "v_smfmac_f32_16x16x64_bf16 v[184:187], v[156:159], v[60:67], v2\n\t" \
    "s_waitcnt lgkmcnt(4)\n\t" \
    "v_smfmac_f32_16x16x64_bf16 v[180:183], v[160:163], v[68:75], v2\n\t" \
    "v_smfmac_f32_16x16x64_bf16 v[184:187], v[160:163], v[76:83], v2\n\t" \
    "s_waitcnt lgkmcnt(3)\n\t" \
    "v_smfmac_f32_16x16x64_bf16 v[180:183], v[164:167], v[84:91], v2\n\t" \
    "s_waitcnt lgkmcnt(2)\n\t" \
    "v_smfmac_f32_16x16x64_bf16 v[180:183], v[168:171], v[100:107], v2\n\t" \
    "s_waitcnt lgkmcnt(1)\n\t" \
    "v_smfmac_f32_16x16x64_bf16 v[180:183], v[172:175], v[116:123], v2\n\t" \
    "s_waitcnt lgkmcnt(0)\n\t" \
    "v_smfmac_f32_16x16x64_bf16 v[180:183], v[176:179], v[132:139], v2\n\t" \
    "v_smfmac_f32_16x16x64_bf16 v[184:187], v[164:167], v[92:99], v2\n\t" \
    "v_smfmac_f32_16x16x64_bf16 v[184:187], v[168:171], v[108:115], v2\n\t" \
    "v_smfmac_f32_16x16x64_bf16 v[184:187], v[172:175], v[124:131], v2\n\t" \
    "v_permlane32_swap_b32 v180, v181\n\t" \
    "v_permlane32_swap_b32 v182, v183\n\t" \
    "v_smfmac_f32_16x16x64_bf16 v[184:187], v[176:179], v[140:147], v2\n\t" \
    "v_add_f32 v180, v180, v181\n\t" \
    "v_add_f32 v182, v182, v183\n\t" \
    "v_cvt_pk_bf16_f32 v180, v180, v182\n\t" \
    "ds_write_b16 v4, v180 offset:0\n\t" \
    "ds_write_b16_d16_hi v4, v180 offset:32\n\t" \
    "s_nop 2\n\t" \
    "v_permlane32_swap_b32 v184, v185\n\t" \
    "v_permlane32_swap_b32 v186, v187\n\t" \
    "v_add_f32 v184, v184, v185\n\t" \
    "v_add_f32 v186, v186, v187\n\t" \
    "v_cvt_pk_bf16_f32 v184, v184, v186\n\t" \
    "ds_write_b16 v4, v184 offset:8\n\t" \
    "ds_write_b16_d16_hi v4, v184 offset:40\n\t" \
    "s_waitcnt lgkmcnt(0)\n\t" \
    "s_barrier\n\t" \
    "s_cmp_eq_u32 s48, 63\n\t" \
    "s_cbranch_scc1 98f\n\t" \
    "ds_write_b8 v15, v1\n\t" \
    "s_mov_b32 s100, 0\n\t" \
    "s_bitcmp0_b32 s51, 0\n\t" \
    "s_cbranch_scc1 83f\n\t" \
    "global_load_dword v14, v13, s[38:39] sc1\n\t" \
    "83:\n\t" \
    "s_memrealtime s[76:77]\n\t" \
    "s_memtime s[28:29]\n\t" \
    "s_add_u32 s63, s70, 2832\n\t" \
    "s_waitcnt lgkmcnt(0)\n\t" \
    "s_mul_i32 s61, s76, 24\n\t" \
    "s_sub_u32 s33, s28, s33\n\t" \
    "s_add_u32 s32, s32, s33\n\t" \
    "s_sub_u32 s32, s32, s61\n\t" \
    "s_max_i32 s32, s32, 0\n\t" \
    "s_min_i32 s32, s32, 95\n\t" \
    "s_add_u32 s32, s32, s61\n\t" \
    "s_mov_b32 s33, s28\n\t" \
    "s_branch 118f\n\t" \
    "116:\n\t" \
    "s_memrealtime s[76:77]\n\t" \
    "s_memtime s[28:29]\n\t" \
    "s_waitcnt lgkmcnt(0)\n\t" \
    "s_mul_i32 s61, s76, 24\n\t" \
    "s_cmp_lg_u32 s76, s30\n\t" \
    "s_cselect_b32 s32, s61, s32\n\t" \
    "s_cselect_b32 s33, s28, s33\n\t" \
    "118:\n\t" \
    "s_mov_b32 s30, s76\n\t" \
    "s_sub_u32 s61, s63, s61\n\t" \
    "s_cmp_gt_i32 s61, 330\n\t" \
    "s_cbranch_scc1 116b\n\t" \
    "s_sub_u32 s61, s63, s32\n\t" \
    "s_add_u32 s61, s61, s33\n\t" \
    "s_sub_u32 s61, s61, s28\n\t" \
    "117:\n\t" \
    "s_sub_u32 s61, s61, 34\n\t" \
    "s_cmp_gt_i32 s61, 100\n\t" \
    "s_cbranch_scc1 117b\n\t" \
    "global_load_dwordx4 v[148:151], v7, s[54:55] sc1\n\t" \
    "global_load_dwordx2 v[152:153], v208, s[54:55] offset:128 sc1\n\t" \
    "global_load_dwordx2 v[154:155], v208, s[54:55] offset:192 sc1\n\t" \
    "global_load_dwordx4 v[156:159], v6, s[58:59] sc1\n\t" \
    "global_load_dwordx4 v[160:163], v6, s[58:59] offset:1024 sc1\n\t" \
    "s_memrealtime s[76:77]\n\t" \
    "s_memtime s[28:29]\n\t" \
    "s_add_u32 s63, s70, 3120\n\t" \
    "s_waitcnt lgkmcnt(0)\n\t" \
    "s_mul_i32 s61, s76, 24\n\t" \
    "s_sub_u32 s33, s28, s33\n\t" \
    "s_add_u32 s32, s32, s33\n\t" \
    "s_sub_u32 s32, s32, s61\n\t" \
    "s_max_i32 s32, s32, 0\n\t" \
    "s_min_i32 s32, s32, 95\n\t" \
    "s_add_u32 s32, s32, s61\n\t" \
    "s_mov_b32 s33, s28\n\t" \
    "s_branch 123f\n\t" \
    "121:\n\t" \
    "s_memrealtime s[76:77]\n\t" \
    "s_memtime s[28:29]\n\t" \
    "s_waitcnt lgkmcnt(0)\n\t" \
    "s_mul_i32 s61, s76, 24\n\t" \
    "s_cmp_lg_u32 s76, s30\n\t" \
    "s_cselect_b32 s32, s61, s32\n\t" \
    "s_cselect_b32 s33, s28, s33\n\t" \
    "123:\n\t" \
    "s_mov_b32 s30, s76\n\t" \
    "s_sub_u32 s61, s63, s61\n\t" \
    "s_cmp_gt_i32 s61, 330\n\t" \
    "s_cbranch_scc1 121b\n\t" \
    "s_sub_u32 s61, s63, s32\n\t" \
    "s_add_u32 s61, s61, s33\n\t" \
    "s_sub_u32 s61, s61, s28\n\t" \
    "s_cmp_lt_i32 s61, -40\n\t" \
    "s_cbranch_scc1 124f\n\t" \
    "125:\n\t" \
    "122:\n\t" \
    "s_sub_u32 s61, s61, 34\n\t" \
    "s_cmp_gt_i32 s61, 100\n\t" \
    "s_cbranch_scc1 122b\n\t" \
    "global_load_dwordx4 v[164:167], v6, s[56:57] sc1\n\t" \
    "global_load_dwordx4 v[168:171], v6, s[56:57] offset:1024 sc1\n\t" \
    "s_waitcnt vmcnt(4)\n\t" \
    "v_max3_u32 v17, v148, v149, v150\n\t" \
    "v_max3_u32 v18, v151, v152, v153\n\t" \
    "v_max3_u32 v17, v17, v154, v155\n\t" \
    "v_max_u32 v17, v17, v18\n\t" \
    "v_cmp_eq_u32 vcc, -1, v17\n\t" \
    "v_lshlrev_b32 v172, 16, v148\n\t" \
    "v_and_b32 v173, 0xffff0000, v148\n\t" \
    "v_lshlrev_b32 v174, 16, v149\n\t" \
    "v_and_b32 v175, 0xffff0000, v149\n\t" \
    "v_lshlrev_b32 v176, 16, v150\n\t" \
    "v_and_b32 v177, 0xffff0000, v150\n\t" \
    "v_lshlrev_b32 v178, 16, v151\n\t" \
    "v_and_b32 v179, 0xffff0000, v151\n\t" \
    "s_cbranch_vccnz 119f\n\t" \
    "120:\n\t" \
    "v_max_f32 v17, v152, v154\n\t" \
    "v_sub_f32 v152, v152, v17\n\t" \
    "v_sub_f32 v154, v154, v17\n\t" \
    "v_exp_f32 v152, v152\n\t" \
    "v_exp_f32 v154, v154\n\t" \
    "v_pk_add_f32 v[180:181], v[172:173], v[174:175]\n\t" \
    "v_pk_add_f32 v[182:183], v[176:177], v[178:179]\n\t" \
    "v_pk_add_f32 v[180:181], v[180:181], v[182:183]\n\t" \
    "v_mul_f32 v17, v154, v155\n\t" \
    "v_add_f32 v153, v180, v181\n\t" \
    "v_fmac_f32 v17, v152, v153\n\t" \
    "v_rcp_f32 v17, v17\n\t" \
    "s_nop 0\n\t" \
    "v_mul_f32 v152, v152, v17\n\t" \
    "v_mul_f32 v18, v154, v17\n\t" \
    "v_pk_mul_f32 v[172:173], v[172:173], v[152:153] op_sel_hi:[1,0]\n\t" \
    "v_pk_mul_f32 v[174:175], v[174:175], v[152:153] op_sel_hi:[1,0]\n\t" \
    "v_pk_mul_f32 v[176:177], v[176:177], v[152:153] op_sel_hi:[1,0]\n\t" \
    "v_pk_mul_f32 v[178:179], v[178:179], v[152:153] op_sel_hi:[1,0]\n\t" \
    "v_cvt_pk_bf16_f32 v18, v18, v18\n\t" \
    "v_cvt_pk_bf16_f32 v148, v172, v173\n\t" \
    "v_cvt_pk_bf16_f32 v149, v174, v175\n\t" \
    "v_cvt_pk_bf16_f32 v150, v176, v177\n\t" \
    "v_cvt_pk_bf16_f32 v151, v178, v179\n\t" \
    "v_and_b32 v180, v18, v196\n\t" \
    "v_and_b32 v181, v18, v197\n\t" \
    "v_and_b32 v182, v18, v198\n\t" \
    "v_and_b32 v183, v18, v199\n\t" \
    "s_waitcnt vmcnt(2)\n\t" \
    "92:\n\t" \
    "v_mfma_f32_16x16x32_bf16 v[188:191], v[156:159], v[148:151], 0\n\t" \
    "v_mfma_f32_16x16x32_bf16 v[192:195], v[160:163], v[148:151], 0\n\t" \
    "v_max3_u32 v17, v156, v157, v158\n\t" \
    "v_max3_u32 v18, v159, v160, v161\n\t" \
    "v_max3_u32 v16, v17, v162, v163\n\t" \
    "v_max_u32 v16, v16, v18\n\t" \
    "s_waitcnt vmcnt(0)\n\t" \
    "v_mfma_f32_16x16x32_bf16 v[184:187], v[164:167], v[180:183], v[188:191]\n\t" \
    "v_mfma_f32_16x16x32_bf16 v[200:203], v[168:171], v[180:183], v[192:195]\n\t" \
    "v_max3_u32 v17, v164, v165, v166\n\t" \
    "v_max3_u32 v18, v167, v168, v169\n\t" \
    "v_max3_u32 v17, v17, v170, v171\n\t" \
    "v_max3_u32 v17, v17, v18, v16\n\t" \
    "v_cmp_eq_u32 vcc, -1, v17\n\t" \
    "s_memtime s[72:73]\n\t" \
    "s_nop 1\n\t" \
    "v_add_f32_dpp v184, v200, v184 row_shl:8 row_mask:0xf bank_mask:0xf bound_ctrl:1\n\t" \
    "v_add_f32_dpp v185, v201, v185 row_shl:8 row_mask:0xf bank_mask:0xf bound_ctrl:1\n\t" \
    "v_add_f32_dpp v186, v202, v186 row_shl:8 row_mask:0xf bank_mask:0xf bound_ctrl:1\n\t" \
    "v_add_f32_dpp v187, v203, v187 row_shl:8 row_mask:0xf bank_mask:0xf bound_ctrl:1\n\t" \
    "s_cbranch_vccnz 96f\n\t" \
    "94:\n\t" \
    "ds_write_b128 v9, v[184:187]\n\t" \
    "s_waitcnt lgkmcnt(0)\n\t" \
    "s_barrier\n\t" \
    "s_bitcmp0_b32 s51, 0\n\t" \
    "s_cbranch_scc1 89f\n\t" \
    "ds_read_b64 v[16:17], v1 offset:16896\n\t" \
    "s_add_u32 s60, s48, 1\n\t" \
    "s_and_b32 s60, s60, 3\n\t" \
    "s_lshl_b32 s60, s60, 10\n\t" \
    "s_add_u32 s74, s38, s60\n\t" \
    "s_addc_u32 s75, s39, 0\n\t" \
    "s_sub_u32 s72, s72, s33\n\t" \
    "s_add_u32 s72, s72, s32\n\t" \
    "s_add_u32 s72, s72, s71\n\t" \
    "s_lshr_b32 s60, s62, 4\n\t" \
    "s_waitcnt lgkmcnt(0)\n\t" \
    "v_readfirstlane_b32 s61, v16\n\t" \
    "v_readfirstlane_b32 s63, v17\n\t" \
    "ds_write_b32 v1, v1 offset:16900\n\t" \
    "s_cmp_lg_u32 s61, 0\n\t" \
    "s_cselect_b32 s73, 1200, 0\n\t" \
    "s_add_u32 s72, s72, s73\n\t" \
    "s_cmp_lg_u32 s63, 0\n\t" \
    "s_cselect_b32 s76, 24, 0\n\t" \
    "s_cmp_lg_u32 s49, 0\n\t" \
    "s_cselect_b32 s76, 0, s76\n\t" \
    "v_cmp_eq_u32 vcc, s48, v14\n\t" \
    "s_cmp_lg_u64 vcc, 0\n\t" \
    "s_cselect_b32 s77, 1, 0\n\t" \
    "s_cselect_b32 s76, 0, s76\n\t" \
    "s_add_u32 s62, s62, s76\n\t" \
    "s_min_u32 s62, s62, 239\n\t" \
    "s_lshr_b32 s60, s62, 4\n\t" \
    "s_andn2_b32 s72, s72, 15\n\t" \
    "s_or_b32 s72, s72, s60\n\t" \
    "v_mov_b32 v17, s72\n\t" \
    "s_andn2_b32 s73, s77, s49\n\t" \
    "s_andn2_b32 s73, s73, s99\n\t" \
    "s_cmp_lg_u32 s63, 0\n\t" \
    "s_cselect_b32 s99, 1, 0\n\t" \
    "s_mov_b64 exec, 15\n\t" \
    "global_store_dword v12, v17, s[74:75] sc1\n\t" \
    "s_and_b32 s60, s48, 3\n\t" \
    "s_lshl_b32 s60, s60, 10\n\t" \
    "s_add_u32 s74, s38, s60\n\t" \
    "s_addc_u32 s75, s39, 0\n\t" \
    "global_store_dword v12, v204, s[74:75] sc1\n\t" \
    "s_mov_b64 exec, -1\n\t" \
    "s_cmp_lg_u32 s61, 0\n\t" \
    "s_cselect_b32 s49, 1, 0\n\t" \
    "s_cmp_eq_u32 s63, 0\n\t" \
    "s_cselect_b32 s76, s49, 0\n\t" \
    "s_or_b32 s76, s76, s73\n\t" \
    "s_cmp_lt_u32 s48, 2\n\t" \
    "s_cselect_b32 s76, 0, s76\n\t" \
    "s_add_u32 s60, s71, 96\n\t" \
    "s_cmp_lg_u32 s76, 0\n\t" \
    "s_cselect_b32 s31, s60, s31\n\t" \
    "s_add_u32 s98, s98, 1\n\t" \
    "s_or_b32 s61, s61, s63\n\t" \
    "s_cselect_b32 s98, 0, s98\n\t" \
    "s_cselect_b32 s63, 0, 1\n\t" \
    "s_cselect_b32 s61, 0, 24\n\t" \
    "s_cmp_gt_u32 s98, 99\n\t" \
    "s_cselect_b32 s60, 36, 14\n\t" \
    "s_cselect_b32 s61, 36, s61\n\t" \
    "s_sub_u32 s31, s31, s60\n\t" \
    "s_max_i32 s31, s31, 0\n\t" \
    "s_min_i32 s31, s31, 1920\n\t" \
    "s_sub_u32 s71, s71, s61\n\t" \
    "s_max_i32 s71, s71, s31\n\t" \
    "s_sub_u32 s62, s62, s63\n\t" \
    "s_max_i32 s62, s62, 0\n\t" \
    "89:\n\t" \
    "s_cmp_eq_u32 s100, 0\n\t" \
    "s_cbranch_scc1 84f\n\t" \
    "s_add_u32 s60, s48, 1\n\t" \
    "v_mov_b32 v17, s60\n\t" \
    "s_mov_b64 exec, 1\n\t" \
    "global_store_dword v13, v17, s[38:39] sc1\n\t" \
    "s_mov_b64 exec, -1\n\t" \
    "84:\n\t" \
    "s_add_u32 s48, s48, 1\n\t" \
    "v_mov_b64 v[180:181], 0\n\t" \
    "v_mov_b64 v[182:183], 0\n\t" \
    "v_mov_b64 v[184:185], 0\n\t" \
    "v_mov_b64 v[186:187], 0\n\t" \
    "s_mov_b32 s60, s48\n\t" \
    "s_and_b32 s60, s60, 3\n\t" \
    "s_add_u32 s61, s60, 4\n\t" \
    "s_lshr_b32 s61, s79, s61\n\t" \
    "s_and_b32 s61, s61, 1\n\t" \
    "s_lshl_b32 s61, s61, 13\n\t" \
    "s_lshl_b32 s77, s60, 19\n\t" \
    "s_or_b32 s61, s61, s77\n\t" \
    "s_add_u32 s58, s46, s61\n\t" \
    "s_addc_u32 s59, s47, 0\n\t" \
    "s_add_u32 s61, s60, 0\n\t" \
    "s_lshr_b32 s61, s79, s61\n\t" \
    "s_and_b32 s61, s61, 1\n\t" \
    "s_lshl_b32 s61, s61, 13\n\t" \
    "s_lshl_b32 s77, s60, 19\n\t" \
    "s_or_b32 s61, s61, s77\n\t" \
    "s_add_u32 s56, s44, s61\n\t" \
    "s_addc_u32 s57, s45, 0\n\t" \
    "s_lshl_b32 s61, s60, 16\n\t" \
    "s_add_u32 s54, s42, s61\n\t" \
    "s_addc_u32 s55, s43, 0\n\t" \
    "s_add_u32 s52, s40, s61\n\t" \
    "s_addc_u32 s53, s41, 0\n\t" \
    "s_lshl_b32 s61, s60, 10\n\t" \
    "s_add_u32 s66, s36, s61\n\t" \
    "s_addc_u32 s67, s37, 0\n\t" \
    "s_and_b32 s60, s70, 15\n\t" \
    "s_mul_i32 s60, s60, 48\n\t" \
    "s_add_u32 s70, s70, s60\n\t" \
    "s_memrealtime s[76:77]\n\t" \
    "s_memtime s[28:29]\n\t" \
    "s_add_u32 s63, s70, 4704\n\t" \
    "s_waitcnt lgkmcnt(0)\n\t" \
    "s_mul_i32 s61, s76, 24\n\t" \
    "s_sub_u32 s33, s28, s33\n\t" \
    "s_add_u32 s32, s32, s33\n\t" \
    "s_sub_u32 s32, s32, s61\n\t" \
    "s_max_i32 s32, s32, 0\n\t" \
    "s_min_i32 s32, s32, 95\n\t" \
    "s_add_u32 s32, s32, s61\n\t" \
    "s_mov_b32 s33, s28\n\t" \
    "s_branch 128f\n\t" \
    "126:\n\t" \
    "s_memrealtime s[76:77]\n\t" \
    "s_memtime s[28:29]\n\t" \
    "s_waitcnt lgkmcnt(0)\n\t" \
    "s_mul_i32 s61, s76, 24\n\t" \
    "s_cmp_lg_u32 s76, s30\n\t" \
    "s_cselect_b32 s32, s61, s32\n\t" \
    "s_cselect_b32 s33, s28, s33\n\t" \
    "128:\n\t" \
    "s_mov_b32 s30, s76\n\t" \
    "s_sub_u32 s61, s63, s61\n\t" \
    "s_cmp_gt_i32 s61, 330\n\t" \
    "s_cbranch_scc1 126b\n\t" \
    "s_sub_u32 s61, s63, s32\n\t" \
    "s_add_u32 s61, s61, s33\n\t" \
    "s_sub_u32 s61, s61, s28\n\t" \
    "127:\n\t" \
    "s_sub_u32 s61, s61, 34\n\t" \
    "s_cmp_gt_i32 s61, 100\n\t" \
    "s_cbranch_scc1 127b\n\t" \
    "129:\n\t" \
    "global_load_dwordx4 v[148:151], v0, s[52:53] sc1\n\t" \
    "global_load_dwordx4 v[152:155], v0, s[52:53] offset:1024 sc1\n\t" \
    "global_load_dword v14, v1, s[66:67] sc1\n\t" \
    "s_sub_u32 s50, s50, 1\n\t" \
    "s_cselect_b32 s50, 0, s50\n\t" \
    "s_cselect_b64 s[74:75], 0, -1\n\t" \
    "s_waitcnt vmcnt(0)\n\t" \
    "ds_write_b128 v11, v[148:151]\n\t" \
    "v_max3_u32 v17, v148, v149, v150\n\t" \
    "v_max3_u32 v18, v151, v152, v153\n\t" \
    "v_max3_u32 v17, v17, v154, v155\n\t" \
    "v_max3_u32 v17, v17, v18, v14\n\t" \
    "v_cmp_eq_u32 vcc, -1, v17\n\t" \
    "ds_write_b128 v11, v[152:155] offset:1024\n\t" \
    "v_readfirstlane_b32 s70, v14\n\t" \
    "s_and_b64 vcc, vcc, s[74:75]\n\t" \
    "s_cbranch_vccnz 130f\n\t" \
    "s_branch 90b\n\t" \
    "124:\n\t" \
    "v_mov_b32 v17, 2\n\t" \
    "ds_write_b8 v15, v17\n\t" \
    "s_mov_b32 s100, 1\n\t" \
    "s_branch 125b\n\t" \
    "119:\n\t" \
    "ds_write_b8 v15, v8\n\t" \
    "s_mov_b32 s100, 1\n\t" \
    "global_load_dwordx4 v[148:151], v7, s[54:55] sc1\n\t" \
    "global_load_dwordx2 v[152:153], v208, s[54:55] offset:128 sc1\n\t" \
    "global_load_dwordx2 v[154:155], v208, s[54:55] offset:192 sc1\n\t" \
    "s_sub_u32 s50, s50, 1\n\t" \
    "s_cselect_b32 s50, 0, s50\n\t" \
    "s_waitcnt vmcnt(0)\n\t" \
    "s_cbranch_scc1 120b\n\t" \
    "v_max3_u32 v17, v148, v149, v150\n\t" \
    "v_max3_u32 v18, v151, v152, v153\n\t" \
    "v_max3_u32 v17, v17, v154, v155\n\t" \
    "v_max_u32 v17, v17, v18\n\t" \
    "v_cmp_eq_u32 vcc, -1, v17\n\t" \
    "v_lshlrev_b32 v172, 16, v148\n\t" \
    "v_and_b32 v173, 0xffff0000, v148\n\t" \
    "v_lshlrev_b32 v174, 16, v149\n\t" \
    "v_and_b32 v175, 0xffff0000, v149\n\t" \
    "v_lshlrev_b32 v176, 16, v150\n\t" \
    "v_and_b32 v177, 0xffff0000, v150\n\t" \
    "v_lshlrev_b32 v178, 16, v151\n\t" \
    "v_and_b32 v179, 0xffff0000, v151\n\t" \
    "s_cbranch_vccnz 119b\n\t" \
    "s_branch 120b\n\t" \
    "96:\n\t" \
    "ds_write_b8 v15, v2\n\t" \
    "s_mov_b32 s100, 1\n\t" \
    "global_load_dwordx4 v[156:159], v6, s[58:59] sc1\n\t" \
    "global_load_dwordx4 v[160:163], v6, s[58:59] offset:1024 sc1\n\t" \
    "global_load_dwordx4 v[164:167], v6, s[56:57] sc1\n\t" \
    "global_load_dwordx4 v[168:171], v6, s[56:57] offset:1024 sc1\n\t" \
    "s_sub_u32 s50, s50, 1\n\t" \
    "s_cselect_b32 s50, 0, s50\n\t" \
    "s_waitcnt vmcnt(0)\n\t" \
    "s_cbranch_scc0 92b\n\t" \
    "s_branch 94b\n\t" \
    "130:\n\t" \
    "ds_write_b8 v15, v8 offset:4\n\t" \
    "s_branch 129b\n\t" \
    "98:\n\t" \
    "s_waitcnt vmcnt(0)\n\t" \
    "s_barrier\n\t" \
    "s_waitcnt vmcnt(0) lgkmcnt(0)\n\t"

#define ASM_H4 \
    "v_mov_b64 v[180:181], 0\n\t" \
    "v_mov_b64 v[182:183], 0\n\t" \
    "v_mov_b64 v[184:185], 0\n\t" \
    "v_mov_b64 v[186:187], 0\n\t" \
    "v_mov_b64 v[188:189], 0\n\t" \
    "v_mov_b64 v[190:191], 0\n\t" \
    "v_mov_b64 v[192:193], 0\n\t" \
    "v_mov_b64 v[194:195], 0\n\t" \
    "s_add_u32 s60, s48, 3\n\t" \
    "s_and_b32 s60, s60, 3\n\t" \
    "s_add_u32 s61, s60, 4\n\t" \
    "s_lshr_b32 s61, s79, s61\n\t" \
    "s_and_b32 s61, s61, 1\n\t" \
    "s_lshl_b32 s61, s61, 13\n\t" \
    "s_lshl_b32 s77, s60, 19\n\t" \
    "s_or_b32 s61, s61, s77\n\t" \
    "s_add_u32 s66, s46, s61\n\t" \
    "s_addc_u32 s67, s47, 0\n\t" \
    "s_add_u32 s61, s60, 0\n\t" \
    "s_lshr_b32 s61, s79, s61\n\t" \
    "s_and_b32 s61, s61, 1\n\t" \
    "s_lshl_b32 s61, s61, 13\n\t" \
    "s_lshl_b32 s77, s60, 19\n\t" \
    "s_or_b32 s61, s61, s77\n\t" \
    "s_add_u32 s68, s44, s61\n\t" \
    "s_addc_u32 s69, s45, 0\n\t" \
    "s_add_u32 s60, s48, 1\n\t" \
    "s_and_b32 s60, s60, 3\n\t" \
    "s_lshl_b32 s61, s60, 16\n\t" \
    "s_add_u32 s52, s40, s61\n\t" \
    "s_addc_u32 s53, s41, 0\n\t" \
    "s_mov_b32 s60, s48\n\t" \
    "s_and_b32 s60, s60, 3\n\t" \
    "s_add_u32 s61, s60, 4\n\t" \
    "s_lshr_b32 s61, s79, s61\n\t" \
    "s_and_b32 s61, s61, 1\n\t" \
    "s_lshl_b32 s61, s61, 13\n\t" \
    "s_lshl_b32 s77, s60, 19\n\t" \
    "s_or_b32 s61, s61, s77\n\t" \
    "s_add_u32 s58, s46, s61\n\t" \
    "s_addc_u32 s59, s47, 0\n\t" \
    "s_lshl_b32 s61, s60, 16\n\t" \
    "s_add_u32 s100, s40, s61\n\t" \
    "s_addc_u32 s101, s41, 0\n\t" \
    "90:\n\t" \
    "s_barrier\n\t" \
    "global_store_dwordx4 v12, v[204:207], s[66:67] offset:0 sc1\n\t" \
    "global_store_dwordx4 v12, v[204:207], s[66:67] offset:1024 sc1\n\t" \
    "global_store_dwordx4 v12, v[204:207], s[68:69] offset:0 sc1\n\t" \
    "global_store_dwordx4 v12, v[204:207], s[68:69] offset:1024 sc1\n\t" \
    "s_barrier\n\t" \
    "ds_read_b128 v[148:151], v13 offset:12800\n\t" \
    "ds_read_b128 v[152:155], v13 offset:13312\n\t" \
    "ds_read_b128 v[156:159], v13 offset:13824\n\t" \
    "ds_read_b128 v[160:163], v13 offset:14336\n\t" \
    "ds_read_b64_tr_b16 v[172:173], v4\n\t" \
    "s_cmp_eq_u32 s48, 63\n\t" \
    "s_cbranch_scc1 93f\n\t" \
    "s_waitcnt lgkmcnt(4)\n\t" \
    "v_smfmac_f32_16x16x64_bf16 v[180:183], v[148:151], v[20:27], v2\n\t" \
    "v_smfmac_f32_16x16x64_bf16 v[184:187], v[148:151], v[52:59], v2\n\t" \
    "s_waitcnt lgkmcnt(3)\n\t" \
    "v_smfmac_f32_16x16x64_bf16 v[180:183], v[152:155], v[28:35], v2\n\t" \
    "s_waitcnt lgkmcnt(2)\n\t" \
    "v_smfmac_f32_16x16x64_bf16 v[180:183], v[156:159], v[36:43], v2\n\t" \
    "s_waitcnt lgkmcnt(1)\n\t" \
    "v_smfmac_f32_16x16x64_bf16 v[180:183], v[160:163], v[44:51], v2\n\t" \
    "v_smfmac_f32_16x16x64_bf16 v[184:187], v[152:155], v[60:67], v2\n\t" \
    "v_smfmac_f32_16x16x64_bf16 v[184:187], v[156:159], v[68:75], v2\n\t" \
    "v_smfmac_f32_16x16x64_bf16 v[184:187], v[160:163], v[76:83], v2\n\t" \
    "v_smfmac_f32_16x16x64_bf16 v[188:191], v[148:151], v[84:91], v2\n\t" \
    "v_smfmac_f32_16x16x64_bf16 v[188:191], v[152:155], v[92:99], v2\n\t" \
    "v_smfmac_f32_16x16x64_bf16 v[188:191], v[156:159], v[100:107], v2\n\t" \
    "v_cvt_pk_bf16_f32 v164, v180, v181\n\t" \
    "v_cvt_pk_bf16_f32 v165, v182, v183\n\t" \
    "v_smfmac_f32_16x16x64_bf16 v[188:191], v[160:163], v[108:115], v2\n\t" \
    "v_cvt_pk_bf16_f32 v166, v184, v185\n\t" \
    "v_cvt_pk_bf16_f32 v167, v186, v187\n\t" \
    "v_smfmac_f32_16x16x64_bf16 v[192:195], v[148:151], v[116:123], v2\n\t" \
    "v_permlane16_swap_b32 v164, v166\n\t" \
    "v_smfmac_f32_16x16x64_bf16 v[192:195], v[152:155], v[124:131], v2\n\t" \
    "v_permlane16_swap_b32 v165, v167\n\t" \
    "v_smfmac_f32_16x16x64_bf16 v[192:195], v[156:159], v[132:139], v2\n\t" \
    "v_min_u32 v164, -2, v164\n\t" \
    "v_min_u32 v166, -2, v166\n\t" \
    "v_smfmac_f32_16x16x64_bf16 v[192:195], v[160:163], v[140:147], v2\n\t" \
    "v_min_u32 v165, -2, v165\n\t" \
    "v_min_u32 v167, -2, v167\n\t" \
    "global_store_dwordx4 v3, v[164:167], s[58:59] sc1\n\t" \
    "v_cvt_pk_bf16_f32 v168, v188, v189\n\t" \
    "v_cvt_pk_bf16_f32 v169, v190, v191\n\t" \
    "s_nop 2\n\t" \
    "v_cvt_pk_bf16_f32 v170, v192, v193\n\t" \
    "v_cvt_pk_bf16_f32 v171, v194, v195\n\t" \
    "s_nop 0\n\t" \
    "v_permlane16_swap_b32 v168, v170\n\t" \
    "v_permlane16_swap_b32 v169, v171\n\t" \
    "v_min_u32 v168, -2, v168\n\t" \
    "v_min_u32 v170, -2, v170\n\t" \
    "v_min_u32 v169, -2, v169\n\t" \
    "v_min_u32 v171, -2, v171\n\t" \
    "global_store_dwordx4 v6, v[168:171], s[58:59] sc1\n\t" \
    "93:\n\t" \
    "s_waitcnt lgkmcnt(0)\n\t" \
    "global_store_dwordx2 v5, v[172:173], s[64:65] sc1\n\t" \
    "s_add_u32 s64, s64, 65536\n\t" \
    "s_addc_u32 s65, s65, 0\n\t" \
    "s_cmp_eq_u32 s48, 0\n\t" \
    "s_cbranch_scc1 131f\n\t" \
    "s_bitcmp0_b32 s51, 1\n\t" \
    "s_cbranch_scc1 131f\n\t" \
    "s_mov_b64 exec, 1\n\t" \
    "global_store_dwordx2 v1, v[196:197], s[38:39] sc1\n\t" \
    "s_mov_b64 exec, -1\n\t" \
    "v_add_u32 v196, 1, v196\n\t" \
    "131:\n\t" \
    "s_cmp_eq_u32 s48, 63\n\t" \
    "s_cbranch_scc1 98f\n\t" \
    "s_setprio 2\n\t" \
    "s_barrier\n\t" \
    "ds_read_b64 v[148:149], v10 offset:8192\n\t" \
    "ds_read_b64 v[150:151], v10 offset:9216\n\t" \
    "ds_read_b64 v[152:153], v10 offset:10240\n\t" \
    "ds_read_b64 v[154:155], v10 offset:11264\n\t" \
    "s_waitcnt lgkmcnt(2)\n\t" \
    "v_pk_add_f32 v[148:149], v[148:149], v[150:151]\n\t" \
    "s_waitcnt lgkmcnt(0)\n\t" \
    "v_pk_add_f32 v[152:153], v[152:153], v[154:155]\n\t" \
    "v_pk_add_f32 v[148:149], v[148:149], v[152:153]\n\t" \
    "v_cvt_pk_bf16_f32 v17, v148, v149\n\t" \
    "v_min_u32 v17, -2, v17\n\t" \
    "global_store_dword v11, v17, s[52:53] sc1\n\t" \
    "global_store_dword v11, v204, s[100:101] sc1\n\t" \
    "s_setprio 0\n\t" \
    "s_waitcnt vmcnt(0)\n\t" \
    "s_add_u32 s48, s48, 1\n\t" \
    "v_mov_b64 v[180:181], 0\n\t" \
    "v_mov_b64 v[182:183], 0\n\t" \
    "v_mov_b64 v[184:185], 0\n\t" \
    "v_mov_b64 v[186:187], 0\n\t" \
    "v_mov_b64 v[188:189], 0\n\t" \
    "v_mov_b64 v[190:191], 0\n\t" \
    "v_mov_b64 v[192:193], 0\n\t" \
    "v_mov_b64 v[194:195], 0\n\t" \
    "s_add_u32 s60, s48, 3\n\t" \
    "s_and_b32 s60, s60, 3\n\t" \
    "s_add_u32 s61, s60, 4\n\t" \
    "s_lshr_b32 s61, s79, s61\n\t" \
    "s_and_b32 s61, s61, 1\n\t" \
    "s_lshl_b32 s61, s61, 13\n\t" \
    "s_lshl_b32 s77, s60, 19\n\t" \
    "s_or_b32 s61, s61, s77\n\t" \
    "s_add_u32 s66, s46, s61\n\t" \
    "s_addc_u32 s67, s47, 0\n\t" \
    "s_add_u32 s61, s60, 0\n\t" \
    "s_lshr_b32 s61, s79, s61\n\t" \
    "s_and_b32 s61, s61, 1\n\t" \
    "s_lshl_b32 s61, s61, 13\n\t" \
    "s_lshl_b32 s77, s60, 19\n\t" \
    "s_or_b32 s61, s61, s77\n\t" \
    "s_add_u32 s68, s44, s61\n\t" \
    "s_addc_u32 s69, s45, 0\n\t" \
    "s_add_u32 s60, s48, 1\n\t" \
    "s_and_b32 s60, s60, 3\n\t" \
    "s_lshl_b32 s61, s60, 16\n\t" \
    "s_add_u32 s52, s40, s61\n\t" \
    "s_addc_u32 s53, s41, 0\n\t" \
    "s_mov_b32 s60, s48\n\t" \
    "s_and_b32 s60, s60, 3\n\t" \
    "s_add_u32 s61, s60, 4\n\t" \
    "s_lshr_b32 s61, s79, s61\n\t" \
    "s_and_b32 s61, s61, 1\n\t" \
    "s_lshl_b32 s61, s61, 13\n\t" \
    "s_lshl_b32 s77, s60, 19\n\t" \
    "s_or_b32 s61, s61, s77\n\t" \
    "s_add_u32 s58, s46, s61\n\t" \
    "s_addc_u32 s59, s47, 0\n\t" \
    "s_lshl_b32 s61, s60, 16\n\t" \
    "s_add_u32 s100, s40, s61\n\t" \
    "s_addc_u32 s101, s41, 0\n\t" \
    "s_branch 90b\n\t" \
    "98:\n\t" \
    "s_waitcnt vmcnt(0)\n\t" \
    "s_barrier\n\t" \
    "s_bitcmp0_b32 s51, 1\n\t" \
    "s_cbranch_scc1 88f\n\t" \
    "s_mov_b64 exec, 1\n\t" \
    "global_store_dwordx2 v1, v[196:197], s[38:39] sc1\n\t" \
    "s_mov_b64 exec, -1\n\t" \
    "88:\n\t" \
    "s_waitcnt vmcnt(0) lgkmcnt(0)\n\t"

typedef u32 u32x16 __attribute__((ext_vector_type(16)));
typedef u32 u32x32 __attribute__((ext_vector_type(32)));
DEV u32x32 cat4(u32x8 a, u32x8 b, u32x8 c, u32x8 d) {
  u32x16 lo = __builtin_shufflevector(a, b, 0, 1, 2, 3, 4, 5, 6, 7, 8, 9, 10, 11, 12, 13, 14, 15);
  u32x16 hi = __builtin_shufflevector(c, d, 0, 1, 2, 3, 4, 5, 6, 7, 8, 9, 10, 11, 12, 13, 14, 15);
  return __builtin_shufflevector(lo, hi, 0, 1, 2, 3, 4, 5, 6, 7, 8, 9, 10, 11, 12, 13, 14, 15, 16, 17, 18, 19, 20, 21, 22, 23, 24, 25, 26, 27, 28, 29, 30, 31);
}
DEV int lane_id() { int l; asm volatile("v_mbcnt_lo_u32_b32 %0, -1, 0\n\tv_mbcnt_hi_u32_b32 %0, -1, %0" : "=v"(l)); return l; }
DEV u32 lo32(const void* p) { return (u32)(size_t)p; }
DEV u32 hi32(const void* p) { return (u32)((size_t)p >> 32); }
#define V10(a) "v" #a "0", "v" #a "1", "v" #a "2", "v" #a "3", "v" #a "4", "v" #a "5", "v" #a "6", "v" #a "7", "v" #a "8", "v" #a "9"
#define S10(a) "s" #a "0", "s" #a "1", "s" #a "2", "s" #a "3", "s" #a "4", "s" #a "5", "s" #a "6", "s" #a "7", "s" #a "8", "s" #a "9"
#define CLOB_HI "v212", "v213", "v214", "v215", "v216", "v217", "v218", "v219", V10(22), V10(23), V10(24), "v250", "v251"
#define ASM_END0 "s_cmp_lg_u32 s50, 0\n\ts_cbranch_scc1 97f\n\tv_mov_b32 v17, 1\n\tglobal_store_dword v1, v17, s[34:35] sc1\n97:\n\t"
#define ASM_END ASM_END0 "s_endpgm\n\t"
#define CLOB_S "s28", "s29", "s30", "s31", "s32", "s33", "s52", "s53", "s54", "s55", "s56", "s57", "s58", "s59", "s60", "s61", "s62", "s63", "s66", "s67", "s70", "s71", "s68", "s69", "s72", "s73", "s74", "s75", "s76", "s77", "s78", "s98", "s99", "s100", "s101", "vcc", "scc", "memory"

DEV u64 ld_word(const Params& p, u64 tag, int idx) { return __hip_atomic_load((const GAS u64*)(p.ws + WS_CTRL) + idx, __ATOMIC_RELAXED, __HIP_MEMORY_SCOPE_AGENT) - tag; }
DEV void set_flag(const Params& p, u64 tag, int idx, u32 val = 0) { __hip_atomic_store((GAS u64*)(p.ws + WS_CTRL) + idx, tag + val, __ATOMIC_RELAXED, __HIP_MEMORY_SCOPE_AGENT); }
template <int SLEEP>
DEV u32 wait_word(const Params& p, u64 tag, int idx) {
  u32 sp = SPIN_CAP;
  u64 v;
  for (;;) { v = ld_word(p, tag, idx); if (!any_lane(v >= 256)) break; if (sp == 0) { set_abort((u32*)(p.ws + WS_CTRL)); break; } --sp; __builtin_amdgcn_s_sleep(SLEEP); }
  asm volatile("" ::: "memory");
  return (u32)v;
}
DEV void wait_flags(const Params& p, u64 tag, int idx, int n, int lane) { wait_word<4>(p, tag, idx + (lane < n ? lane : 0)); }
DEV u32 get_pol(const Params& p, u64 tag, int lane) {
  const u32 pol = (u32)__builtin_amdgcn_ballot_w64(wait_word<2>(p, tag, C_POL + (lane < 10 ? lane : 0)) == 1) & 0x3FFu;
  wait_word<2>(p, tag, C_ARM + lane); wait_word<2>(p, tag, C_ARM + 64 + lane);
  return pol;
}
DEV void probe_window(const Params& p, u64 tag, int j, int w) {
  char* base = p.ws + WS_BASE + (size_t)j * 524288 + w * 512;
  u32 best0 = ~0u, best1 = ~0u;
  if (lane_id() == 0) {
    for (int t = 0; t < 8; ++t) {
      char* q = base + ((t ^ w) & 1) * 8192 + (t >> 1) * 128; u32 v = SENT;
      u64 c0 = __builtin_amdgcn_s_memtime();
      asm volatile("s_waitcnt lgkmcnt(0)\n\tglobal_store_dword %0, %1, off sc1\n\ts_waitcnt vmcnt(0)" :: "v"(q), "v"(v) : "memory");
      u32 d = (u32)(__builtin_amdgcn_s_memtime() - c0);
      if ((t ^ w) & 1) best1 = min(best1, d); else best0 = min(best0, d);
    }
    *(LAS u32x2*)(size_t)(147456 + w * 8) = u32x2{best0, best1};
  }
  asm volatile("s_waitcnt lgkmcnt(0)\n\ts_barrier" ::: "memory");
  if (w == 0 && lane_id() == 0) {
    for (int k = 1; k < 8; ++k) { u32x2 o = *(LAS u32x2*)(size_t)(147456 + k * 8); best0 = min(best0, o.x); best1 = min(best1, o.y); }
    set_flag(p, tag, C_POL + j, best1 < best0 ? 1u : 0u);
  }
}
#define POLB(k) (((pol >> (k)) & 1) * 8192)
#define ROLE_END(unit) __builtin_unreachable()
#define IO4(a, b, c, d) :: "{v[0:15]}"(a), "{v[16:19]}"(b), "{s[48:51]}"(c), "{s[64:65]}"(d),
#define COMMON_IN "{s79}"(pol), "{s[34:35]}"(ws + WS_CTRL + C_ABORT * 8), "{s[40:47]}"(sa)

DEV void main_x(const Params& p, u64 tag, int h, int x, int w) {
  int lane = lane_id();
  int g = lane >> 4, l15 = lane & 15;
  char* ws = p.ws;
  u32x8 W1[2][8], W2[2][4];
  const u16* m0p = p.mem0 + (l15 & 7) * DM + 128 * w + 16 * g + 8 * (l15 >> 3);
  *(LAS u32x4*)(size_t)(M_OFF + w * 2048 + lane * 16) = *(const GAS u32x4*)m0p;
  *(LAS u32x4*)(size_t)(M_OFF + w * 2048 + 1024 + lane * 16) = *(const GAS u32x4*)(m0p + 64);
  {
    u32x4 r1[8][4], r2[2][8];
    w_issue<2, 8>(r1, p.Wq + h * DH + 32 * w, NH * DH, lane, 4 * w);
    w_issue<4, 2>(r2, p.Wo + (size_t)h * DH * DM + 64 * w, DM, lane, w);
    w_convert<2, 8>(W1, r1, WSC_OFF + w * 8192, lane);
    w_convert2(W2, r2, WSC_OFF + w * 8192, lane);
  }
  asm volatile("" : "+v"(lane));
  g = lane >> 4; l15 = lane & 15;
  const int tl = T_OFF + w * 2048 + lane * 16;
#pragma unroll
  for (int k = 0; k < 4; ++k) {
    u32x8 v = W1[k & 1][6 + (k >> 1)];
    *(LAS u32x4*)(size_t)(tl + k * 8192) = u32x4{v[0], v[1], v[2], v[3]};
    *(LAS u32x4*)(size_t)(tl + k * 8192 + 1024) = u32x4{v[4], v[5], v[6], v[7]};
  }
  const int orow = (g * 8 + (lane & 7)) * 16;
  u32x16 va;
  va[0] = lane * 16; va[1] = 0; va[2] = (l15 < 8) ? 0x4444 : 0xEEEE;
  va[3] = ((w + 1) & 3) * 512 + orow;
  va[4] = M_OFF + ((w + 1) & 3) * 2048 + lane * 16;
  va[5] = M_OFF + ((w + 2) & 3) * 2048 + lane * 16;
  va[6] = Q_OFF + (4 * (g & 1) + (lane >> 5)) * QS + (32 * w + 8 * (l15 >> 2) + (lane & 3)) * 2;
  va[7] = (l15 & 7) * QS + g * 16;
  va[8] = l15 < 8 ? O_OFF + (w * 32 + g * 8 + l15) * 16 : DUMP_OFF + lane * 16;
  va[9] = M_OFF + ((w + 3) & 3) * 2048 + lane * 16;
  va[10] = lane * 8;
  va[11] = M_OFF + w * 2048 + lane * 16;
  va[12] = ((w + 2) & 3) * 512 + orow;
  va[13] = ((4 * w + (((g & 1) << 1) | (g >> 1))) * 64 + h) * 256 + l15 * 16;
  va[14] = KV_OFF + lane * 16;
  va[15] = KV_OFF + 8192 + w * 2048 + lane * 16;
  u32x4 vb4 = {(u32)(((w + 3) & 3) * 512 + orow), 0u, 0u, 0u};
  const u32 pol = get_pol(p, tag, lane);
  const char *mb = ws + WS_MB + POLB(8) + x * 16384 + w * 2048, *tb = ws + WS_TB + POLB(9) + x * 256, *pb = ws + WS_PB, *sb = ws + WS_SB + POLB(8) + w * 16384 + h * 256;
  u32x8 sa = {lo32(mb), hi32(mb), lo32(tb), hi32(tb), lo32(pb), hi32(pb), lo32(sb), hi32(sb)};
  u32x4 sc = {0u, 0u, SPIN_CAP, (u32)(8192 + w * 2048)};
  u64 sd = 0;
  asm volatile("s_mov_b32 s62, 0x42400000\n\t"
               ASM_X ASM_END IO4(va, vb4, sc, sd) COMMON_IN,
                 "{v[20:51]}"(cat4(W1[0][0], W1[1][0], W1[0][1], W1[1][1])), "{v[52:83]}"(cat4(W1[0][2], W1[1][2], W1[0][3], W1[1][3])),
                 "{v[84:115]}"(cat4(W1[0][4], W1[1][4], W1[0][5], W1[1][5])), "{v[116:147]}"(cat4(W2[0][0], W2[0][1], W2[0][2], W2[0][3])), "{v[148:179]}"(cat4(W2[1][0], W2[1][1], W2[1][2], W2[1][3]))
               : V10(18), V10(19), V10(20), "v210", "v211", CLOB_HI, CLOB_S);
  ROLE_END(3 * h);
}

DEV void main_m(const Params& p, u64 tag, int h, int w) {
  int lane = lane_id();
  char* ws = p.ws;
  u32x8 W2[2][4];
  const char* run = ws + (w < 6 ? WS_KF : WS_VF) + (size_t)h * 8192 + (w & 1) * 4096;
  {
    u32x4 r2[2][8];
    w_issue<4, 2>(r2, p.Wo + (size_t)h * DH * DM + 64 * w, DM, lane, 0);
    w_convert2(W2, r2, WSC_OFF + w * 8192, lane);
  }
  const char* kvf = ws + WS_CTRL + (C_KVF + 64 + h * 2 + ((w >> 1) & 1)) * 8;
  wait_flags(p, tag, C_KV2 + h * 4 + (w & 2), 2, lane);
#pragma unroll
  for (int u = 0; u < 4; ++u) GLDS16(run + lane * 16 + u * 1024, KV_OFF + (w - 4) * 4096 + u * 1024);
  const int g = lane >> 4, l15 = lane & 15;
  u32x16 va = {};
  va[0] = lane * 16; va[2] = (l15 < 8) ? 0x4444 : 0xEEEE;
  va[3] = ((4 * w + (((g & 1) << 1) | (g >> 1))) * 64 + h) * 256 + l15 * 16;
  va[7] = lane * 16 + 4096;
  va[10] = (g * 8 + (lane & 7)) * 16;
  u32x4 vb4 = {0u, 0u, 0u, (u32)(STAMP_OFF + w * 8192)};
  const u32 pol = get_pol(p, tag, lane);
  const char* pb = ws + WS_PB;
  const char* kv2 = ws + WS_CTRL + (C_KV2 + h * 4 + (w & 2)) * 8;
  u32x8 sa = {lo32(kv2), hi32(kv2), lo32(kvf), hi32(kvf), lo32(pb), hi32(pb), (u32)tag, (u32)(tag >> 32)};
  u32x4 sc = {0u, 0u, SPIN_CAP, (u32)(KV_OFF + (w - 4) * 4096)};
  asm volatile("s_waitcnt vmcnt(0) lgkmcnt(0)\n\t" ASM_M ASM_END IO4(va, vb4, sc, run) COMMON_IN,
                 "{v[20:51]}"(cat4(W2[0][0], W2[0][1], W2[0][2], W2[0][3])), "{v[52:83]}"(cat4(W2[1][0], W2[1][1], W2[1][2], W2[1][3]))
               : "v84", "v85", "v86", "v87", "v88", "v89", V10(9), V10(10), "v110", "v111", CLOB_S);
  ROLE_END(3 * h);
}

template <int ROLE>
DEV void key_w(const Params& p, u64 tag, int h, int x, int w) {
  int lane = lane_id();
  char* ws = p.ws;
  u32x8 W1[16];
  *(LAS u32x4*)(size_t)(M_OFF + w * 1024 + lane * 16) = *(const GAS u32x4*)(p.mem0 + (lane & 7) * DM + 64 * w + 16 * (lane >> 4) + 8 * ((lane >> 3) & 1));
  {
    u32x4 r1[8][4];
    w_issue<2, 8>(r1, (ROLE < 4 ? p.Wq : p.Wk) + h * DH + 32 * (w & 3), NH * DH, lane, 0);
    w_convert3(W1, r1, WSC_OFF + w * 8192, lane);
  }
  const int g = lane >> 4, l15 = lane & 15;
  u32x16 va;
  va[0] = lane * 16; va[1] = 0; va[2] = (l15 < 8) ? 0x4444 : 0xEEEE;
  va[3] = g * 16384 + l15 * 16;
  va[4] = (ROLE < 4 ? Q_OFF : K_OFF) + (4 * (g & 1)) * QS + (32 * (w & 3) + 8 * (l15 >> 2) + 4 * (lane >> 5) + (lane & 3)) * 2;
  va[9] = (g >> 1) * 16384 + l15 * 16 + (g & 1) * 8;
  va[5] = va[9] + 32768;
  va[6] = g * 16384 + 128 + l15 * 8;
  va[7] = ROLE == 0 ? (l15 & 7) * QS + g * 16 : (lane & 7) * QS + (lane >> 3) * 16;
  va[8] = 1; va[10] = fu(KSCALE); va[11] = fu(KSCALE);
  va[12] = M_OFF + w * 2048 + lane * 16;
  va[13] = M_OFF + (16 * g + 8 * (g & 1) + (lane & 7)) * 16; va[14] = va[13] ^ 128; va[15] = 0;
  u32x4 vb4 = {0u, 0u, 0u, (u32)(STAMP_OFF + w * 8192)};
  const u32 pol = get_pol(p, tag, lane);
  const char *mb = ws + WS_MB + POLB(8) + x * 16384 + w * 2048, *tb = ws + WS_TB + POLB(9) + x * 256, *cnt = ws + WS_CTRL + C_KMF * 8 + h * 128, *sb = ws + WS_SB + POLB(8) + h * 256;
  const char* run = ws + WS_KM + h * 2048;
  u32x8 sa = {lo32(mb), hi32(mb), lo32(tb), hi32(tb), lo32(cnt), hi32(cnt), lo32(sb), hi32(sb)};
  u32x4 sc = {0u, 0u, SPIN_CAP, 0u};
#define K_PRE "v_mov_b32 v192, s36\n\tv_mov_b32 v193, s37\n\tv_mov_b32 v188, -1\n\tv_mov_b32 v189, -1\n\tv_mov_b32 v190, -1\n\tv_mov_b32 v191, -1\n\ts_mov_b32 s62, 0x42400000\n\ts_mov_b32 s58, 0x00ff00ff\n\ts_mov_b32 s59, 0x00ff00ff\n\t"
#define K_OPS ASM_END IO4(va, vb4, sc, run) COMMON_IN, "{s[36:37]}"(tag + 1), "{v[20:51]}"(cat4(W1[0], W1[1], W1[2], W1[3])), "{v[52:83]}"(cat4(W1[4], W1[5], W1[6], W1[7])), \
                "{v[84:115]}"(cat4(W1[8], W1[9], W1[10], W1[11])), "{v[116:147]}"(cat4(W1[12], W1[13], W1[14], W1[15])) \
              : "v148", "v149", V10(15), V10(16), V10(17), V10(18), "v190", "v191", "v192", "v193", CLOB_S
  if (ROLE == 0) asm volatile(K_PRE ASM_K0 K_OPS);
  else if (ROLE == 1) asm volatile(K_PRE ASM_K1 K_OPS);
  else if (ROLE == 4) asm volatile(K_PRE ASM_K4 K_OPS);
  else asm volatile(K_PRE ASM_K5 K_OPS);
  ROLE_END(3 * h + 2);
}

template <int ROLE>
DEV void val_w(const Params& p, u64 tag, int h, int x, int w) {
  int lane = lane_id();
  char* ws = p.ws;
  u32x32 W[4];
  if (ROLE == 0) {
    const u16* m0p = p.mem0 + (lane & 7) * DM + 128 * w + 16 * (lane >> 4) + 8 * ((lane >> 3) & 1);
    *(LAS u32x4*)(size_t)(M_OFF + w * 2048 + lane * 16) = *(const GAS u32x4*)m0p;
    *(LAS u32x4*)(size_t)(M_OFF + w * 2048 + 1024 + lane * 16) = *(const GAS u32x4*)(m0p + 64);
    u32x8 W1[2][8];
    u32x4 r1[8][4];
    w_issue<2, 8>(r1, p.Wv + h * DH + 32 * w, NH * DH, lane, 4 * w);
    w_convert<2, 8>(W1, r1, WSC_OFF + w * 8192, lane);
#pragma unroll
    for (int k = 0; k < 4; ++k) W[k] = cat4(W1[0][2 * k], W1[1][2 * k], W1[0][2 * k + 1], W1[1][2 * k + 1]);
  } else {
    u32x8 Wa[2][4], Wb[2][4];
    u32x4 ra[2][8], rb[2][8];
    w_issue<4, 2>(ra, p.Wo + (size_t)h * DH * DM + 128 * (w & 3), DM, lane, 0);
    w_issue<4, 2>(rb, p.Wo + (size_t)h * DH * DM + 128 * (w & 3) + 64, DM, lane, 0);
    w_convert2(Wa, ra, WSC_OFF + w * 8192, lane);
    w_convert2(Wb, rb, WSC_OFF + w * 8192, lane);
    W[0] = cat4(Wa[0][0], Wa[0][1], Wa[0][2], Wa[0][3]); W[1] = cat4(Wa[1][0], Wa[1][1], Wa[1][2], Wa[1][3]);
    W[2] = cat4(Wb[0][0], Wb[0][1], Wb[0][2], Wb[0][3]); W[3] = cat4(Wb[1][0], Wb[1][1], Wb[1][2], Wb[1][3]);
  }
  const int g = lane >> 4, l15 = lane & 15, wq = w & 3;
  u32x16 va;
  va[0] = lane * 16; va[1] = 0; va[2] = (l15 < 8) ? 0x4444 : 0xEEEE;
  if (ROLE == 0) {
    va[3] = M_OFF + ((w + 1) & 3) * 2048 + lane * 16;
    va[4] = O_OFF + (w * 32 + (l15 >> 2) * 8 + 4 * (g & 1) + (lane >> 5)) * 16 + (lane & 3) * 2;
    va[5] = M_OFF + ((w + 2) & 3) * 2048 + lane * 16;
    va[6] = (h * 64 + 8 * w) * 256 + lane * 16;
    va[10] = M_OFF + ((w + 3) & 3) * 2048 + lane * 16;
    va[11] = M_OFF + w * 2048 + lane * 16;
    va[12] = lane * 256;
  } else {
    va[3] = ((8 * wq + (((g & 1) << 1) | (g >> 1))) * 64 + h) * 256 + l15 * 16;
    va[4] = O_OFF + (wq * 32 + (lane & 3) * 8 + 4 * (g & 1) + (l15 >> 2)) * 16 + (g >> 1) * 8;
    va[5] = wq * 512 + lane * 8;
    va[6] = va[3] + 4 * 16384;
    va[10] = (l15 & 7) * 64 + (l15 >> 3) * 32 + g * 8;
    va[11] = l15 * 16 + g * 4;
    va[12] = h * 16384 + wq * 2048 + lane * 16;
  }
  va[7] = (8 * w + 4 * (l15 >> 3) + g) * 256 + (l15 & 7) * 16;
  va[8] = 1;
  va[9] = RED_OFF + w * 1024 + l15 * 64 + g * 16;
  va[13] = ROLE == 0 ? 4096 + ((h + lane) & 31) * 128 : (g * 8 + (lane & 7)) * 16; va[14] = 0; va[15] = ST_OFF + w;
  const u32 hm = (lane & 1) ? 0xFFFF0000u : 0x0000FFFFu;
  const int dw = (l15 >> 1) & 3;
  u32x4 mk = {dw == 0 ? hm : 0u, dw == 1 ? hm : 0u, dw == 2 ? hm : 0u, dw == 3 ? hm : 0u};
  if (ROLE == 4) mk = u32x4{(u32)tag + 1, (u32)(tag >> 32), 0u, 0u};
  u32x4 vb4 = {0u, 0u, 0u, (u32)(STAMP_OFF + w * 8192)};
  const u32 pol = get_pol(p, tag, lane);
  const int cp = ROLE == 0 ? x : (w & 3);
  const char* mb = ws + WS_MB + POLB(8) + cp * 16384 + (ROLE == 0 ? w * 2048 : h * 256);
  const char *sb = ws + WS_SB + POLB(8) + x * 16384, *pb = ws + WS_PB, *vb = ws + WS_VB, *tb = ws + WS_TB + POLB(9) + cp * 256, *cnt = ROLE == 0 ? ws + WS_TB + POLB(9) : ws + WS_CTRL + C_KMF * 8 + (32 + h) * 128;
  const char* run = ws + WS_VM + h * 2048;
  u32x8 sa = {lo32(mb), hi32(mb), lo32(sb), hi32(sb), lo32(pb), hi32(pb), lo32(vb), hi32(vb)};
  u32x4 se = {lo32(tb), hi32(tb), lo32(cnt), hi32(cnt)};
  u32x4 sc = {0u, 0u, SPIN_CAP, ((u32)(256 - h - 32 * w) >> 8) | (((u32)(12 - w) >> 3) << 1)};
#define H_PRE "v_and_b32 v208, 0x70, v7\n\tv_lshrrev_b32 v208, 1, v208\n\tv_and_b32 v209, 0xffffff00, v7\n\tv_or_b32 v208, v208, v209\n\tv_mov_b32 v204, -1\n\tv_mov_b32 v205, -1\n\tv_mov_b32 v206, -1\n\tv_mov_b32 v207, -1\n\t"
#define H_OPS ASM_END IO4(va, vb4, sc, run) COMMON_IN, "{s[36:39]}"(se), "{v[20:51]}"(W[0]), "{v[52:83]}"(W[1]), "{v[84:115]}"(W[2]), "{v[116:147]}"(W[3]), "{v[196:199]}"(mk) \
              : "v148", "v149", V10(15), V10(16), V10(17), V10(18), "v190", "v191", "v192", "v193", "v194", "v195", V10(20), CLOB_S
  if (ROLE == 0) asm volatile(H_PRE ASM_H0 H_OPS);
  else asm volatile(H_PRE ASM_H4 H_OPS);
  ROLE_END(3 * h + 1);
}

typedef float f32x16 __attribute__((ext_vector_type(16)));
DEV f32x16 mfma3216(u32x4 a, u32x4 b, f32x16 c) { return __builtin_amdgcn_mfma_f32_32x32x16_bf16(__builtin_bit_cast(bf16x8, a), __builtin_bit_cast(bf16x8, b), c, 0, 0, 0); }
DEV float lo16(u32 v) { return uf(v << 16); }
DEV float hi16(u32 v) { return uf(v & 0xffff0000u); }
DEV float rbf(float a) { return lo16(pk(a, a)); }
#define RSRC(ws) __builtin_amdgcn_make_buffer_rsrc((void*)(ws), 0, 0x10000000, 0x00020000)
DEV u32x4 ld16(__amdgpu_buffer_rsrc_t r, u32 voff, u32 soff) { return __builtin_bit_cast(u32x4, __builtin_amdgcn_raw_buffer_load_b128(r, voff, soff, 16)); }
DEV u32x2 ld8(__amdgpu_buffer_rsrc_t r, u32 voff, u32 soff) { return __builtin_bit_cast(u32x2, __builtin_amdgcn_raw_buffer_load_b64(r, voff, soff, 16)); }
DEV void st16(__amdgpu_buffer_rsrc_t r, u32x4 v, u32 voff, u32 soff) { __builtin_amdgcn_raw_buffer_store_b128(v, r, voff + soff, 0, 16); }
#define GLDS16P(gptr, sbase, IMM) asm volatile("s_add_u32 m0, %1, %2\n\ts_nop 0\n\tglobal_load_lds_dwordx4 %0, off" :: "v"(gptr), "s"(sbase), "i"(IMM) : "memory", "scc")
#define BAR() asm volatile("s_waitcnt lgkmcnt(0)\n\ts_barrier" ::: "memory")

struct P1Src { const u16* a; int rs; };
DEV void p1_src(const Params& p, int cls, int pos, int w, int lane, const u16*& s0, const u16*& s1, int& rs) {
  const int q = lane >> 4, cp = lane & 15;
  const int ch0 = cp ^ ((q << 2) | ((2 * w) & 3)), ch1 = cp ^ ((q << 2) | ((2 * w + 1) & 3));
  const int r0 = 8 * w + q, r1 = r0 + 4;
  if (pos < 24) {
    const int hh = (pos < 16 ? (pos >> 1) : pos - 16) * 4 + cls;
    const u16* W = (pos >= 16 ? p.Wq : (pos & 1) ? p.Wv : p.Wk) + hh * DH;
    rs = NH * DH; s0 = W + r0 * rs + ch0 * 8; s1 = W + r1 * rs + ch1 * 8;
  } else {
    const int fb = (pos - 24) * 4 + cls;
    rs = FF;
    s0 = (ch0 < 8 ? p.wg : p.wu) + fb * 64 + r0 * rs + (ch0 & 7) * 8; s1 = (ch1 < 8 ? p.wg : p.wu) + fb * 64 + r1 * rs + (ch1 & 7) * 8;
  }
}
DEV int p1_flag(int rb, int cls, int pos) {
  if (pos < 16) return C_KVF + (rb * 32 + (pos >> 1) * 4 + cls) * 2 + (pos & 1);
  if (pos < 24) return C_QF + rb * 32 + (pos - 16) * 4 + cls;
  return C_HF + rb * 32 + (pos - 24) * 4 + cls;
}

DEV void ln_phase(const Params& p, u64 tag, int grp0, int gs, int w8) {
  const int lane = lane_id(), w = w8 & 3, grp = grp0 + (w8 >> 2) * gs;
  const int ST = (w8 >> 2) * 16640;
  {
    u32x4 v[4];
#pragma unroll
    for (int r = 0; r < 4; ++r) v[r] = *(const GAS u32x4*)(p.x + (size_t)(grp * 16 + 4 * w + r) * DM + lane * 8);
    float f[4][8], sm[4], sq[4];
#pragma unroll
    for (int r = 0; r < 4; ++r) {
#pragma unroll
      for (int j = 0; j < 4; ++j) { f[r][2 * j] = lo16(v[r][j]); f[r][2 * j + 1] = hi16(v[r][j]); }
      sm[r] = ((f[r][0] + f[r][1]) + (f[r][2] + f[r][3])) + ((f[r][4] + f[r][5]) + (f[r][6] + f[r][7]));
    }
#pragma unroll
    for (int o = 1; o < 64; o <<= 1)
#pragma unroll
      for (int r = 0; r < 4; ++r) sm[r] += __shfl_xor(sm[r], o);
#pragma unroll
    for (int r = 0; r < 4; ++r) {
      sm[r] *= (1.f / DM); sq[r] = 0.f;
#pragma unroll
      for (int j = 0; j < 8; ++j) sq[r] = fmaf(f[r][j] - sm[r], f[r][j] - sm[r], sq[r]);
    }
#pragma unroll
    for (int o = 1; o < 64; o <<= 1)
#pragma unroll
      for (int r = 0; r < 4; ++r) sq[r] += __shfl_xor(sq[r], o);
#pragma unroll
    for (int r = 0; r < 4; ++r) {
      const float den = rbf(sqrtf(rbf(rbf(sq[r] * (1.f / DM)) + 1e-5f))), mb = rbf(sm[r]);
      u32x4 o4;
#pragma unroll
      for (int j = 0; j < 4; ++j) o4[j] = pk(rbf(f[r][2 * j] - mb) / den, rbf(f[r][2 * j + 1] - mb) / den);
      *(LAS u32x4*)(size_t)(ST + (4 * w + r) * 1040 + lane * 16) = o4;
    }
  }
  BAR();
  {
    const __amdgpu_buffer_rsrc_t rsrc = RSRC(p.ws);
#pragma unroll
    for (int r = 0; r < 4; ++r) {
      const int c = w * 16 + r * 4 + (lane >> 4), tok = lane & 15;
      st16(rsrc, *(LAS u32x4*)(size_t)(ST + tok * 1040 + c * 16), (c >> 1) * 1024 + ((c & 1) * 32 + (grp & 1) * 16 + tok) * 16, (u32)WS_XF + (grp >> 1) * 32768);
    }
    asm volatile("s_waitcnt vmcnt(0)" ::: "memory");
  }
  BAR();
  if (w == 0 && lane == 0) set_flag(p, tag, C_XF + grp);
}

#define P1_ISSUE(kk) do { GLDS16P(s0, w2k, (kk) * 16384); GLDS16P(s1, w2k, (kk) * 16384 + 1024); s0 += 64 * rs; s1 += 64 * rs; asm volatile("" : "+v"(s0), "+v"(s1)); } while (0)
#define P1_RD(g) do { _Pragma("unroll") for (int i_ = 0; i_ < 2; ++i_) { const int o_ = (k & 3) * 16384 + ((g) / (NT / 2)) * 4096, e_ = 2 * ((g) % (NT / 2)) + i_; \
      u32x2 lo_ = trd(o_ + ta[2 * e_]), hi_ = trd(o_ + ta[2 * e_ + 1]); wf[(g) & 1][i_] = u32x4{lo_.x, lo_.y, hi_.x, hi_.y}; } } while (0)
#define P1_LOOP(VK) _Pragma("unroll") for (int k = 0; k < 8; ++k) { \
      asm volatile("s_cmp_lt_u32 %0, 4\n\ts_cbranch_scc1 1f\n\ts_cmp_lt_u32 %0, 8\n\ts_cbranch_scc1 2f\n\ts_cmp_lt_u32 %0, 9\n\ts_cbranch_scc1 3f\n\ts_waitcnt vmcnt(0)\n1:\n\ts_waitcnt vmcnt(10)\n2:\n\ts_waitcnt vmcnt(14)\n3:\n\ts_waitcnt vmcnt(18)" \
                   :: "s"(NT == 2 ? 64 : k < 2 ? nst : k < 6 ? (nst | tl) : tl) : "memory", "scc"); \
      if (NT == 4 || k == 0) BAR(); \
      if (k == 6 && pend >= 0) { if (w == 0 && lane_id() == 0) set_flag(p, tag, pend); pend = -1; } \
      if (k == 2 && more) p1_src(p, cls, pos + stride, w, lane_id(), s0, s1, rs); \
      if (NT == 4 && (k < 2 || more)) P1_ISSUE((k + 6) & 7); \
      if ((k & 3) == 0) { _Pragma("unroll") for (int t = 0; t < 2 * NT; ++t) { ta[t] = (ta[t] & 0xffff) | ((k >> 2) << 16); asm volatile("" : "+v"(ta[t])); } } \
      u32x4 wf[2][2]; \
      P1_RD(0); \
      _Pragma("unroll") for (int g = 0; g < 2 * NT; ++g) { \
        if (g < 2 * NT - 1) P1_RD(g + 1); \
        SB0(); \
        _Pragma("unroll") for (int i = 0; i < 2; ++i) { const int e = 2 * (g % (NT / 2)) + i, ks = 4 * k + g / (NT / 2); acc[e] = VK ? mfma3216(X[ks], wf[g & 1][i], acc[e]) : mfma3216(wf[g & 1][i], X[ks], acc[e]); } \
        SB0(); \
      } }

DEV void fast_phase(const Params& p, u64 tag, int cls, int pos, int half, int w) {
  char* ws = p.ws;
  const __amdgpu_buffer_rsrc_t rsrc = RSRC(ws);
  const int lane = lane_id(), r31 = lane & 31, hh = lane >> 5;
  constexpr int XL = 65536, RD = 65536, SS = 147456;
  const bool isV = pos & 1;
  const int hd = (pos >> 1) * 4 + cls, sg = w & 1, kq = w >> 1, w8k = w * 8192;
  const int wg = (cls * 2 + half) * 16 + pos;
  {
    const u32x4 sent = {SENT, SENT, SENT, SENT};
    for (int k = wg * 8 + w; k < (int)((WS_INIT_END - WS_BASE) / 1024); k += 1024) GST4(k * 1024 + lane * 16, sent, ws + WS_BASE);
  }
#pragma unroll
  for (int r = 0; r < 8; ++r) GLDS16P(p.x + (size_t)(8 * w + r) * DM + ((lane ^ ((8 * (w & 1) + r) & 15)) * 8), w8k, XL + r * 1024);
  {
    const int r = lane >> 3;
    const u16* src = (isV ? p.Wv : p.Wk) + hd * DH + half * 64 + (size_t)(64 * w + r) * (NH * DH) + ((lane & 7) ^ (2 * (r & 3)) ^ (r >> 2)) * 8;
#pragma unroll
    for (int k = 0; k < 8; ++k) GLDS16P(src + (size_t)k * 8 * (NH * DH), w8k, k * 1024);
  }
  u32x4 X[8];
  {
    asm volatile("s_waitcnt vmcnt(8)" ::: "memory");
    BAR();
    if (w == 0 && lane == 0) set_flag(p, tag, C_ARM + wg);
#pragma unroll
    for (int k = 0; k < 8; ++k) X[k] = *(LAS u32x4*)(size_t)(XL + (sg * 32 + r31) * 1024 + (((16 * kq + 2 * k + hh) ^ (r31 & 15)) * 16));
    float s1a = 0.f, s2a = 0.f, s1b = 0.f, s2b = 0.f;
    const bf16x2 one = __builtin_bit_cast(bf16x2, 0x3F803F80u);
#pragma unroll
    for (int k = 0; k < 8; ++k)
#pragma unroll
      for (int j = 0; j < 4; j += 2) {
        const u32 ua = X[k][j], ub = X[k][j + 1];
        bf16x2 a = __builtin_bit_cast(bf16x2, ua), b = __builtin_bit_cast(bf16x2, ub);
        s1a = __builtin_amdgcn_fdot2_f32_bf16(a, one, s1a, false); s2a = __builtin_amdgcn_fdot2_f32_bf16(a, a, s2a, false);
        s1b = __builtin_amdgcn_fdot2_f32_bf16(b, one, s1b, false); s2b = __builtin_amdgcn_fdot2_f32_bf16(b, b, s2b, false);
      }
    f32x2 st = {s1a + s1b, s2a + s2b};
    st = f32x2{hsum32(st.x, st.x), hsum32(st.y, st.y)};
    *(LAS f32x2*)(size_t)(SS + w * 512 + lane * 8) = st;
    BAR();
    st = (*(LAS f32x2*)(size_t)(SS + sg * 512 + lane * 8) + *(LAS f32x2*)(size_t)(SS + (sg + 2) * 512 + lane * 8)) +
         (*(LAS f32x2*)(size_t)(SS + (sg + 4) * 512 + lane * 8) + *(LAS f32x2*)(size_t)(SS + (sg + 6) * 512 + lane * 8));
    const float mean = st.x * (1.f / DM);
    const float var = fmaxf(st.y * (1.f / DM) - mean * mean, 0.f);
    const float rstd = 1.f / rbf(sqrtf(rbf(rbf(var) + 1e-5f)));
    const f32x2 r2 = {rstd, rstd}, nb = {-rbf(mean) * rstd, -rbf(mean) * rstd};
#pragma unroll
    for (int k = 0; k < 8; ++k)
#pragma unroll
      for (int j = 0; j < 4; ++j) { f32x2 v = f32x2{lo16(X[k][j]), hi16(X[k][j])} * r2 + nb; X[k][j] = pk(v.x, v.y); }
    asm volatile("s_waitcnt vmcnt(0)" ::: "memory");
    BAR();
  }
  u32x4 Wf[2][8];
  {
    const int q = (lane & 15) >> 2, pp = lane & 3, blk = (lane >> 4) & 1;
#pragma unroll
    for (int ct = 0; ct < 2; ++ct) {
      const int t0 = 128 * (128 * kq + 8 * hh + q) + 16 * ((4 * ct + 2 * blk + (pp >> 1)) ^ (2 * q)) + 8 * (pp & 1);
      const int t1 = 128 * (128 * kq + 8 * hh + 4 + q) + 16 * ((4 * ct + 2 * blk + (pp >> 1)) ^ (2 * q) ^ 1) + 8 * (pp & 1);
#pragma unroll
      for (int k = 0; k < 8; ++k) { u32x2 lo = trd(t0 + k * 2048), hi = trd(t1 + k * 2048); Wf[ct][k] = u32x4{lo.x, lo.y, hi.x, hi.y}; }
    }
  }
#pragma nounroll
  for (int rnd = 0; rnd < 8; ++rnd) {
    f32x16 acc[2] = {};
    const int nseg = rnd < 7 ? 2 * rnd + 2 + sg : 14;
    if (isV) {
#pragma unroll
      for (int k = 0; k < 8; ++k) { acc[0] = mfma3216(X[k], Wf[0][k], acc[0]); acc[1] = mfma3216(X[k], Wf[1][k], acc[1]); }
    } else {
#pragma unroll
      for (int k = 0; k < 8; ++k) { acc[0] = mfma3216(Wf[0][k], X[k], acc[0]); acc[1] = mfma3216(Wf[1][k], X[k], acc[1]); }
    }
    SB0();
    if (rnd > 0) {
#pragma unroll
      for (int k = 0; k < 8; ++k) X[k] = ld16(rsrc, lane * 16, (u32)WS_XF + (nseg * 32 + 8 * kq + k) * 1024);
    }
    SB0();
    const int rd = RD + (rnd & 1) * 65536 - (rnd & 1) * 131072;
#pragma unroll
    for (int c = 0; c < 4; ++c) {
      const int t = c >> 1, a = c & 1;
      *(LAS f32x4*)(size_t)(rd + ((sg * 4 + c) * 4 + kq) * 2048 + lane * 16) = f32x4{acc[t][4 * a], acc[t][4 * a + 1], acc[t][4 * a + 2], acc[t][4 * a + 3]};
      *(LAS f32x4*)(size_t)(rd + ((sg * 4 + c) * 4 + kq) * 2048 + 1024 + lane * 16) = f32x4{acc[t][8 + 4 * a], acc[t][9 + 4 * a], acc[t][10 + 4 * a], acc[t][11 + 4 * a]};
    }
    if (rnd > 1) asm volatile("s_waitcnt vmcnt(8)" ::: "memory");
    BAR();
    if (rnd > 1 && w == 0 && lane == 0) set_flag(p, tag, C_KV2 + (((rnd - 1) * 32 + hd) * 2 + isV) * 2 + half);
    f32x4 u0 = {}, u1 = {};
#pragma unroll
    for (int v = 0; v < 4; ++v) {
      u0 += *(LAS f32x4*)(size_t)(rd + ((sg * 4 + kq) * 4 + v) * 2048 + lane * 16);
      u1 += *(LAS f32x4*)(size_t)(rd + ((sg * 4 + kq) * 4 + v) * 2048 + 1024 + lane * 16);
    }
    const float sc = isV ? 1.f : KSCALE;
    const u32 es = isV ? 2048 : 1024;
    st16(rsrc, u32x4{pk(u0[0] * sc, u0[1] * sc), pk(u0[2] * sc, u0[3] * sc), pk(u1[0] * sc, u1[1] * sc), pk(u1[2] * sc, u1[3] * sc)},
         (isV ? (r31 >> 4) * 1024 : (r31 >> 4) * 4096) + hh * 256 + (lane & 15) * 16 + (kq & 1) * 512,
         (u32)(isV ? WS_VF : WS_KF) + ((2 * rnd + sg) * NH + hd) * 8192 + (2 * half + (kq >> 1)) * es);
    if (rnd == 0 || rnd == 7) {
      asm volatile("s_waitcnt vmcnt(0)" ::: "memory");
      BAR();
      if (w == 0 && lane == 0) set_flag(p, tag, C_KV2 + ((rnd * 32 + hd) * 2 + isV) * 2 + half);
      if (rnd == 0) {
        wait_flags(p, tag, C_XF, 32, lane);
#pragma unroll
        for (int k = 0; k < 8; ++k) X[k] = ld16(rsrc, lane * 16, (u32)WS_XF + (nseg * 32 + 8 * kq + k) * 1024);
      }
    }
  }
}

template <int NT>
DEV void p1_phase(const Params& p, u64 tag, int seg0, int cls, int pos0, int pos1, int stride, int nurgent, int w) {
  char* ws = p.ws;
  const __amdgpu_buffer_rsrc_t rsrc = RSRC(ws);
  const u16 *s0, *s1; int rs;
  const int w2k = w * 2048;
  p1_src(p, cls, pos0, w, lane_id(), s0, s1, rs);
  BAR();
  const int seg = seg0 + (NT == 4 ? w : (w & 3)), e0 = NT == 4 ? 0 : (w >> 2) * 2, rb = seg0 >> 3;
  u32x4 X[32];
  int ta[2 * NT];
  {
    const int lane = lane_id(), hh = lane >> 5, q = (lane & 15) >> 2, pp = lane & 3, blk = (lane >> 4) & 1;
    if (NT == 4) {
#pragma unroll
      for (int k = 0; k < 6; ++k) P1_ISSUE(k);
      wait_flags(p, tag, C_XF + 2 * seg, 2, lane);
#pragma unroll
      for (int k = 0; k < 32; ++k) X[k] = ld16(rsrc, lane * 16, (u32)WS_XF + (seg * 32 + k) * 1024);
    } else {
      const u16* xr = p.x + (size_t)(seg * 32 + (lane & 31)) * DM + 8 * hh;
#pragma unroll
      for (int k = 0; k < 32; ++k) asm volatile("global_load_dwordx4 %0, %1, off offset:%2" : "=v"(X[k]) : "v"(xr), "i"(32 * k) : "memory");
#pragma unroll
      for (int k = 0; k < 8; ++k) P1_ISSUE(k);
      asm volatile("s_waitcnt vmcnt(16)" ::: "memory");
#pragma unroll
      for (int k = 0; k < 32; k += 4) asm volatile("" : "+v"(X[k]), "+v"(X[k + 1]), "+v"(X[k + 2]), "+v"(X[k + 3]));
      float s1a = 0.f, s2a = 0.f, s1b = 0.f, s2b = 0.f;
      const bf16x2 one = __builtin_bit_cast(bf16x2, 0x3F803F80u);
#pragma unroll
      for (int k = 0; k < 32; ++k)
#pragma unroll
        for (int j = 0; j < 4; j += 2) {
          const u32 ua = X[k][j], ub = X[k][j + 1];
          bf16x2 a = __builtin_bit_cast(bf16x2, ua), b = __builtin_bit_cast(bf16x2, ub);
          s1a = __builtin_amdgcn_fdot2_f32_bf16(a, one, s1a, false); s2a = __builtin_amdgcn_fdot2_f32_bf16(a, a, s2a, false);
          s1b = __builtin_amdgcn_fdot2_f32_bf16(b, one, s1b, false); s2b = __builtin_amdgcn_fdot2_f32_bf16(b, b, s2b, false);
        }
      float sm = s1a + s1b, sq = s2a + s2b;
      sm = hsum32(sm, sm); sq = hsum32(sq, sq);
      const float mean = sm * (1.f / DM);
      const float var = fmaxf(sq * (1.f / DM) - mean * mean, 0.f);
      const float rstd = 1.f / rbf(sqrtf(rbf(rbf(var) + 1e-5f)));
      const f32x2 r2 = {rstd, rstd}, nb = {-rbf(mean) * rstd, -rbf(mean) * rstd};
#pragma unroll
      for (int k = 0; k < 32; ++k)
#pragma unroll
        for (int j = 0; j < 4; ++j) { f32x2 v = f32x2{lo16(X[k][j]), hi16(X[k][j])} * r2 + nb; X[k][j] = pk(v.x, v.y); }
    }
#pragma unroll
    for (int t = 0; t < 2 * NT; ++t) ta[t] = 256 * (8 * hh + 4 * (t & 1) + q) + 16 * ((2 * blk + (pp >> 1)) ^ (2 * hh + (t & 1))) + 8 * (pp & 1) + 64 * ((e0 + (t >> 1)) ^ q);
#pragma unroll
    for (int k = 0; k < 32; k += 4) asm volatile("" : "+v"(X[k]), "+v"(X[k + 1]), "+v"(X[k + 2]), "+v"(X[k + 3]));
  }
  int pend = -1, nst = 0, ucount = 0;
  for (int pos = pos0; pos < pos1; pos += stride, ++ucount) {
    const bool isV = pos < 16 && (pos & 1);
    const bool more = pos + stride < pos1;
    const int tl = more ? 0 : 64;
    f32x16 acc[NT];
#pragma unroll
    for (int e = 0; e < NT; ++e)
#pragma unroll
      for (int i = 0; i < 16; ++i) acc[e][i] = 0.f;
    if (isV) { P1_LOOP(true) } else { P1_LOOP(false) }
    const int lane = lane_id(), r31 = lane & 31, hh = lane >> 5;
    if (NT == 2 || pos < 24) {
      const int hd = (pos < 16 ? (pos >> 1) : pos - 16) * 4 + cls;
      const float sc = (pos < 16 && !isV) ? KSCALE : 1.f;
      const u32 es = isV ? 2048 : 1024;
      const u32 sbase = (u32)(pos >= 16 ? WS_QF : isV ? WS_VF : WS_KF) + (seg * NH + hd) * 8192 + e0 * es;
      const u32 vo = (isV ? (r31 >> 4) * 1024 : (r31 >> 4) * 4096) + hh * 256 + (lane & 15) * 16;
#pragma unroll
      for (int e = 0; e < NT; ++e)
#pragma unroll
        for (int a = 0; a < 2; ++a) {
          u32x4 c = {pk(acc[e][4 * a] * sc, acc[e][4 * a + 1] * sc), pk(acc[e][4 * a + 2] * sc, acc[e][4 * a + 3] * sc),
                     pk(acc[e][8 + 4 * a] * sc, acc[e][9 + 4 * a] * sc), pk(acc[e][10 + 4 * a] * sc, acc[e][11 + 4 * a] * sc)};
          st16(rsrc, c, vo + a * 512, sbase + e * es);
        }
      nst = 8;
    } else {
      const u32 sbase = (u32)WS_H + (seg * 112 + ((pos - 24) * 4 + cls) * 4) * 1024;
#pragma unroll
      for (int ft = 0; ft < 2; ++ft)
#pragma unroll
        for (int s2 = 0; s2 < 2; ++s2) {
          u32x4 c;
#pragma unroll
          for (int j = 0; j < 4; ++j) {
            u32 gg = pk(acc[ft][8 * s2 + 2 * j], acc[ft][8 * s2 + 2 * j + 1]), uu = pk(acc[NT - 2 + ft][8 * s2 + 2 * j], acc[NT - 2 + ft][8 * s2 + 2 * j + 1]);
            c[j] = pk(lo16(gg) * lo16(uu), hi16(gg) * hi16(uu));
          }
          st16(rsrc, c, lane * 16, sbase + (ft * 2 + s2) * 1024);
        }
      nst = 4;
    }
    pend = p1_flag(rb, cls, pos);
    if (!more || ucount < nurgent) {
      asm volatile("s_waitcnt vmcnt(0)" ::: "memory");
      BAR();
      if (w == 0 && lane == 0) set_flag(p, tag, pend);
      pend = -1; nst = 0;
    }
  }
}

template <int NS>
DEV void slab_load(u32x4 (&Wf)[NS], const u16* base, int scr, int lane) {
  const int hh = lane >> 5, q = (lane & 15) >> 2, pp = lane & 3, blk = (lane >> 4) & 1;
  const int ra = scr + (4 * hh + q) * 64 + blk * 32 + pp * 8;
#pragma unroll
  for (int k = 0; k < NS; ++k) Wf[k] = *(const GAS u32x4*)(base + (size_t)(16 * k + (lane >> 2)) * DM + (lane & 3) * 8);
  SB0();
#pragma unroll
  for (int k0 = 0; k0 < NS; k0 += 8) {
#pragma unroll
    for (int k = k0; k < k0 + 8 && k < NS; ++k) *(LAS u32x4*)(size_t)(scr + (k - k0) * 1024 + lane * 16) = Wf[k];
#pragma unroll
    for (int k = k0; k < k0 + 8 && k < NS; ++k) { u32x2 lo = trd(ra + (k - k0) * 1024), hi = trd(ra + (k - k0) * 1024 + 512); Wf[k] = u32x4{lo.x, lo.y, hi.x, hi.y}; }
    asm volatile("s_waitcnt lgkmcnt(0)" ::: "memory");
  }
}
DEV f32x2 tile_reduce(const f32x16& acc, int red, int w, int lane) {
#pragma unroll
  for (int r = 0; r < 8; ++r) *(LAS f32x2*)(size_t)(red + (w * 8 + r) * 512 + lane * 8) = f32x2{acc[2 * r], acc[2 * r + 1]};
  BAR();
  f32x2 s = *(LAS f32x2*)(size_t)(red + w * 512 + lane * 8);
#pragma unroll
  for (int v = 1; v < 8; ++v) s += *(LAS f32x2*)(size_t)(red + (v * 8 + w) * 512 + lane * 8);
  return s;
}

DEV void out_phase(const Params& p, u64 tag, int c, int j, int w) {
  const int lane = lane_id();
  char* ws = p.ws;
  const __amdgpu_buffer_rsrc_t rsrc = RSRC(ws);
  constexpr int GL = 131072;
  int par = 0;
  BAR();
  {
    u32x4 Wf[14], H[14];
    slab_load<14>(Wf, p.wd + (size_t)w * 224 * DM + j * 32, 65536 + w * 8192, lane);
#pragma unroll
    for (int k = 0; k < 4; ++k) wait_word<4>(p, tag, C_HF + k * 64 + ((lane & 31) < 28 ? lane : 0));
#pragma unroll
    for (int k = 0; k < 14; ++k) H[k] = ld16(rsrc, lane * 16, (u32)WS_H + (c * 112 + w * 14 + k) * 1024);
    SB0();
    int gl = GL + (w * 64 + lane) * 4;
    for (int seg = c; seg < NSEG; seg += 6, par ^= 1, gl += 2048) {
      f32x16 acc = {};
      const int nx = seg + 6 < NSEG ? seg + 6 : seg;
#pragma unroll
      for (int k = 0; k < 14; ++k) { acc = mfma3216(H[k], Wf[k], acc); H[k] = ld16(rsrc, lane * 16, (u32)WS_H + (nx * 112 + w * 14 + k) * 1024); SB0(); }
      f32x2 s = tile_reduce(acc, par * 32768, w, lane);
      const float a = rbf(s.x), b = rbf(s.y);
      *(LAS u32*)(size_t)gl = pk(a / (1.f + __expf(-a)), b / (1.f + __expf(-b)));
    }
  }
  u32x4 Wf[32];
  slab_load<32>(Wf, p.Wo + (size_t)w * 512 * DM + j * 32, 65536 + w * 8192, lane);
  const int row0 = ((2 * w) & 3) + 8 * (w >> 1) + 4 * (lane >> 5), col = j * 32 + (lane & 31);
  const int fl = C_OF + 4 * w + (lane & 3);
  u32x4 O[16];
  bool have = false;
  int gl = GL + (w * 64 + lane) * 4;
  for (int seg = c; seg < NSEG; seg += 6, par ^= 1, gl += 2048) {
    const u32 sb = (u32)WS_O + (seg * 256 + w * 32) * 1024;
    const size_t o = (size_t)(seg * 32 + row0) * DM + col;
    const u16 x0 = p.x[o], x1 = p.x[o + DM];
    if (!have) {
      if (seg < NSEG - 3) wait_word<OSL>(p, tag, fl + seg * 32); else wait_word<1>(p, tag, fl + seg * 32);
#pragma unroll
      for (int k = 0; k < 16; ++k) O[k] = ld16(rsrc, lane * 16, sb + k * 1024);
    }
    const u64 nf = ld_word(p, tag, fl + (seg + 6 < NSEG ? seg + 6 : seg) * 32);
    SB0();
    f32x16 acc = {};
#pragma unroll
    for (int k = 0; k < 16; ++k) { acc = mfma3216(O[k], Wf[k], acc); O[k] = ld16(rsrc, lane * 16, sb + (16 + k) * 1024); SB0(); }
    have = seg + 6 < NSEG && !any_lane(nf >= 256);
    asm volatile("" ::: "memory");
    if (have) {
#pragma unroll
      for (int k = 0; k < 16; ++k) { acc = mfma3216(O[k], Wf[16 + k], acc); O[k] = ld16(rsrc, lane * 16, sb + 6 * 256 * 1024 + k * 1024); SB0(); }
    } else {
#pragma unroll
      for (int k = 0; k < 16; ++k) acc = mfma3216(O[k], Wf[16 + k], acc);
    }
    f32x2 s = tile_reduce(acc, par * 32768, w, lane);
    const u32 gv = *(LAS u32*)(size_t)gl;
    u32 r = pk(bf2f(x0) + rbf(rbf(s.x) * lo16(gv)), bf2f(x1) + rbf(rbf(s.y) * hi16(gv)));
    p.out[o] = (u16)r; p.out[o + DM] = (u16)(r >> 16);
  }
  if (c == (NSEG - 1) % 6 && j == 0 && w == 0 && lane == 0) set_flag(p, tag, C_EPOCH);
}

DEV void attn_phase(const Params& p, u64 tag, int seg0, int hd) {
  const int lane = lane_id();
  char* ws = p.ws;
  const __amdgpu_buffer_rsrc_t rsrc = RSRC(ws);
  const int r31 = lane & 31, hh = lane >> 5;
  const u32 vq = (r31 >> 4) * 4096 + hh * 256 + (lane & 15) * 16, vv = (r31 >> 4) * 1024 + hh * 256 + (lane & 15) * 16;
  int pend = -1;
  for (int seg = seg0; seg < NSEG; seg += 8) {
    const int rb = seg >> 3;
    const int fq = (lane < 4 ? (rb < 2 ? C_KV2 + ((seg >> 1) * 32 + hd) * 4 + lane : C_KVF + (rb * 32 + hd) * 2 + (lane & 1))
                                                          : lane == 4 ? C_QF + rb * 32 + hd : C_KMF + ((lane & 1) * 32 + hd) * 16);
    bool late;
    {
      u32 sp = SPIN_CAP;
      for (;;) {
        u64 v = ld_word(p, tag, fq);
        if (pend >= 0) { asm volatile("s_waitcnt vmcnt(0)" ::: "memory"); if (lane == 0) set_flag(p, tag, pend); pend = -1; }
        late = any_lane(lane > 4 && (v >= 256 || v <= (u64)seg));
        if (!any_lane(lane <= 4 && v >= 256)) break;
        if (sp == 0) { set_abort((u32*)(ws + WS_CTRL)); break; }
        --sp; __builtin_amdgcn_s_sleep(4);
      }
      asm volatile("" ::: "memory");
    }
    const u32 so = (seg * NH + hd) * 8192, sm = (seg * NH + hd) * 2048;
    u32x4 Q[8], K[8], V[8], KM[8];
    u32x2 VM[4];
#pragma unroll
    for (int k = 0; k < 8; ++k) { Q[k] = ld16(rsrc, vq + (k & 1) * 512, (u32)WS_QF + so + (k >> 1) * 1024); K[k] = ld16(rsrc, vq + (k & 1) * 512, (u32)WS_KF + so + (k >> 1) * 1024); }
#pragma unroll
    for (int k = 0; k < 8; ++k) V[k] = ld16(rsrc, vv + (k & 1) * 512, (u32)WS_VF + so + (k >> 1) * 2048);
#define ATT_MEM() do { _Pragma("unroll") for (int k = 0; k < 8; ++k) KM[k] = ld16(rsrc, hh * 128 + (lane & 7) * 16 + (k & 1) * 256, (u32)WS_KM + sm + (k >> 1) * 512); \
      _Pragma("unroll") for (int e = 0; e < 4; ++e) VM[e] = ld8(rsrc, (r31 >> 4) * 256 + hh * 128 + (lane & 15) * 8, (u32)WS_VM + sm + e * 512); } while (0)
    if (!late) ATT_MEM();
    SB0();
    f32x16 sx = {};
#pragma unroll
    for (int k = 0; k < 8; ++k) sx = mfma3216(K[k], Q[k], sx);
    float mx = -3.0e38f;
#pragma unroll
    for (int i = 0; i < 16; ++i) { const int key = (i & 3) + 8 * (i >> 2) + 4 * hh; sx[i] = key <= r31 ? sx[i] : -1.0e9f * 1.4426950408889634f; mx = fmaxf(mx, sx[i]); }
    if (late) {
      u32 sp = SPIN_CAP;
      for (;;) { u64 v = ld_word(p, tag, fq); if (!any_lane(lane > 4 && (v >= 256 || v <= (u64)seg))) break; if (sp == 0) { set_abort((u32*)(ws + WS_CTRL)); break; } --sp; if (seg < NSEG - 2) __builtin_amdgcn_s_sleep(OSL); else __builtin_amdgcn_s_sleep(1); }
      asm volatile("" ::: "memory");
      ATT_MEM();
    }
    SB0();
    f32x16 sm_ = {};
#pragma unroll
    for (int k = 0; k < 8; ++k) sm_ = mfma3216(KM[k], Q[k], sm_);
    float em[4];
#pragma unroll
    for (int i = 0; i < 4; ++i) { em[i] = sm_[i] * KSCALE; mx = fmaxf(mx, em[i]); }
    mx = xmx32(mx);
    float z = 0.f;
#pragma unroll
    for (int i = 0; i < 4; ++i) { em[i] = __builtin_amdgcn_exp2f(em[i] - mx); z += em[i]; }
#pragma unroll
    for (int i = 0; i < 16; ++i) { sx[i] = __builtin_amdgcn_exp2f(sx[i] - mx); z += sx[i]; }
    const float inv = 1.f / hsum32(z, z);
    const u32x4 pm = {pk(em[0] * inv, em[1] * inv), pk(em[2] * inv, em[3] * inv), 0u, 0u};
    u32x4 p0, p1;
#pragma unroll
    for (int i = 0; i < 4; ++i) { p0[i] = pk(sx[2 * i] * inv, sx[2 * i + 1] * inv); p1[i] = pk(sx[8 + 2 * i] * inv, sx[9 + 2 * i] * inv); }
    const u32 ob = (u32)WS_O + (seg * 256 + hd * 8) * 1024;
#pragma unroll
    for (int e = 0; e < 4; ++e) {
      f32x16 o = {};
      o = mfma3216(u32x4{V[2 * e].x, V[2 * e].y, V[2 * e + 1].x, V[2 * e + 1].y}, p0, o);
      o = mfma3216(u32x4{V[2 * e].z, V[2 * e].w, V[2 * e + 1].z, V[2 * e + 1].w}, p1, o);
      o = mfma3216(u32x4{VM[e].x, VM[e].y, 0u, 0u}, pm, o);
#pragma unroll
      for (int s2 = 0; s2 < 2; ++s2)
        st16(rsrc, u32x4{pk(o[8 * s2], o[8 * s2 + 1]), pk(o[8 * s2 + 2], o[8 * s2 + 3]), pk(o[8 * s2 + 4], o[8 * s2 + 5]), pk(o[8 * s2 + 6], o[8 * s2 + 7])}, lane * 16, ob + (e * 2 + s2) * 1024);
    }
    pend = C_OF + seg * 32 + hd;
    if (late || seg + 8 >= NSEG) { asm volatile("s_waitcnt vmcnt(0)" ::: "memory"); if (lane == 0) set_flag(p, tag, pend); pend = -1; }
  }
}

__global__ void __launch_bounds__(512, 2) fused_kernel(Params p) {
  extern __shared__ __attribute__((aligned(16))) char smem[];
  u64 last;
  asm volatile("s_load_dwordx2 %0, %1, 0x8\n\ts_waitcnt lgkmcnt(0)" : "=s"(last) : "s"(p.ws));
  const u64 tag = ((last >> 8) + 1) << 8;
  const int b = blockIdx.x, x = b & 7, y = b >> 3;
  const int w = __builtin_amdgcn_readfirstlane(threadIdx.x >> 6);
  if (x < 4 && y < 24) {
    const int h = (y & 7) * 4 + x;
    if (y < 8) { if (w < 4) main_x(p, tag, h, x, w); else main_m(p, tag, h, w); }
    else if (y < 16) { if (w < 4) val_w<0>(p, tag, h, x, w); else val_w<4>(p, tag, h, x, w); }
    else { if (w == 0) key_w<0>(p, tag, h, x, w); else if (w < 4) key_w<1>(p, tag, h, x, w); else if (w == 4) key_w<4>(p, tag, h, x, w); else key_w<5>(p, tag, h, x, w); }
    return;
  }
  const int cls = x & 3;
  if (x < 4) {
    const int i = (y - 24) * 4 + x;
    if (i >= 22) probe_window(p, tag, i - 22, w); else if (i < 16) ln_phase(p, tag, i, 16, w);
  }
  if (x >= 4) fast_phase(p, tag, cls, y & 15, y >> 4, w);
  else { const int i = (y - 24) * 4 + x; ln_phase(p, tag, 32 + i, 32, w); if (i >= 16) ln_phase(p, tag, 80 + i, 16, w); }
#pragma nounroll
  for (int round = 0; round < 2; ++round) {
    int rb, pos0, pos1 = 31, stride, urg = 0;
    if (x < 4) { rb = 2 - 2 * round; pos0 = round * 16 + y - 24; pos1 = 31 - 7 * round; stride = 8; urg = 2; }
    else if (y >= 30) { if (round) break; rb = 0; pos0 = y - 6; stride = 2; }
    else if (round == 0) { rb = 3 + y / 6; pos0 = y % 6; stride = 6; }
    else { const int k = (y / 6) * 5 + y % 6 - 1; if (y % 6 == 0 || k >= 15) break; rb = 1; pos0 = 16 + k; stride = 16; }
    p1_phase<4>(p, tag, rb * 8, cls, pos0, pos1, stride, urg, w);
  }
  if (x < 4) return;
  if (y < 24) out_phase(p, tag, (cls * 24 + y) >> 4, (cls * 24 + y) & 15, w);
  else attn_phase(p, tag, cls + 4 * ((y - 24) >> 2), (y & 3) * 8 + w);
}

extern "C" void kernel_launch(void* const* d_in, const int* in_sizes, int n_in, void* d_out, int out_size, void* d_ws, size_t ws_size, hipStream_t stream) {
  Params p;
  p.x = (const u16*)d_in[0]; p.mem0 = (const u16*)d_in[1]; p.Wq = (const u16*)d_in[2]; p.Wk = (const u16*)d_in[3]; p.Wv = (const u16*)d_in[4]; p.Wo = (const u16*)d_in[5];
  p.wg = (const u16*)d_in[8]; p.wu = (const u16*)d_in[9]; p.wd = (const u16*)d_in[10];
  p.out = (u16*)d_out; p.ws = (char*)d_ws;
  (void)hipFuncSetAttribute((const void*)fused_kernel, hipFuncAttributeMaxDynamicSharedMemorySize, LDS_BYTES);
  fused_kernel<<<256, 512, LDS_BYTES, stream>>>(p);
}
```
